```python
import torch

def _softmax(x, axis=-1):
    return torch.softmax(x, dim=axis)
def _gelu(x):
    return torch.nn.functional.gelu(x, approximate="tanh")
def _silu(x):
    return torch.nn.functional.silu(x)
def _sigmoid(x):
    return torch.sigmoid(x)
def _layernorm(x, axis=-1, eps=1e-5):
    m = x.mean(dim=axis, keepdim=True)
    v = x.var(dim=axis, keepdim=True, unbiased=False)
    return (x - m) / torch.sqrt(v + eps)
def _scatter_add(tgt, idx, src, axis=0):
    return tgt.clone().index_add_(axis, idx, src)
def _segment_sum(src, seg, n):
    out = torch.zeros((n,) + src.shape[1:], dtype=src.dtype)
    return out.index_add_(0, seg, src)
def _pad(x, pw):
    flat = tuple(v for pair in reversed(pw) for v in pair)
    return torch.nn.functional.pad(x, flat)

kernel_name = 'standalone__dense_activation_gelu__prompt'

def setup_inputs():

    _w = (lambda a: (lambda t: t.to(torch.bfloat16) if t.is_floating_point() else t)(torch.as_tensor(a)))
    B, S, D, F = 1, 2048, 512, 1376
    return {
        "x": _w((lambda *s: torch.randn(*s, dtype=torch.bfloat16) * 0.05)(B, S, D)),
        "w1": _w((lambda *s: torch.randn(*s, dtype=torch.bfloat16) * 3.2)(D, F) * (D ** -0.5)),
        "b1": _w((lambda *s: torch.randn(*s, dtype=torch.bfloat16) * 3.2)(F) * 0.01),
        "w2": _w((lambda *s: torch.randn(*s, dtype=torch.bfloat16) * 0.05)(F, D) * (F ** -0.5)),
        "b2": _w((lambda *s: torch.randn(*s, dtype=torch.bfloat16) * 0.1)(D) * 0.01),
    }

def reference(x, w1, b1, w2, b2):
    h = torch.einsum("bsd,df->bsf", x, w1) + b1
    h = _gelu(h)
    return torch.einsum("bsf,fd->bsd", h, w2) + b2
```

```cpp
#include <hip/hip_runtime.h>
#include <hip/hip_bf16.h>
#include <stdint.h>

typedef __attribute__((ext_vector_type(8))) short bf16x8;
typedef __attribute__((ext_vector_type(16))) float f32x16;
typedef unsigned short u16;

__device__ __forceinline__ float bf2f(u16 v) { return __uint_as_float(((unsigned)v) << 16); }
__device__ __forceinline__ u16 f2bf(float f) { __hip_bfloat16 b = __float2bfloat16(f); return *(u16*)&b; }

template <bool GELU>
__global__ __launch_bounds__(256) void gemm_simple(const u16* __restrict__ A, const u16* __restrict__ B,
                                                   const u16* __restrict__ bias, u16* __restrict__ C,
                                                   int M, int N, int K) {
    __shared__ __attribute__((aligned(16))) u16 As[64][40];
    __shared__ __attribute__((aligned(16))) u16 Bs[64][40];
    const int tid = threadIdx.x, lane = tid & 63, wid = tid >> 6;
    const int wr = wid >> 1, wc = wid & 1;
    const int m0 = blockIdx.y * 64, n0 = blockIdx.x * 64;
    f32x16 acc;
    for (int i = 0; i < 16; ++i) acc[i] = 0.f;
    for (int k0 = 0; k0 < K; k0 += 32) {
        {
            int r = tid >> 2, c = (tid & 3) * 8;
            bf16x8 v = *(const bf16x8*)(A + (size_t)(m0 + r) * K + k0 + c);
            *(bf16x8*)&As[r][c] = v;
        }
        {
            int kr = tid >> 3, c = (tid & 7) * 8;
            bf16x8 v;
            if (n0 + c < N) v = *(const bf16x8*)(B + (size_t)(k0 + kr) * N + n0 + c);
            else for (int j = 0; j < 8; ++j) v[j] = 0;
            for (int j = 0; j < 8; ++j) Bs[c + j][kr] = (u16)v[j];
        }
        __syncthreads();
        for (int s = 0; s < 2; ++s) {
            bf16x8 a = *(const bf16x8*)&As[wr * 32 + (lane & 31)][s * 16 + 8 * (lane >> 5)];
            bf16x8 b = *(const bf16x8*)&Bs[wc * 32 + (lane & 31)][s * 16 + 8 * (lane >> 5)];
            acc = __builtin_amdgcn_mfma_f32_32x32x16_bf16(a, b, acc, 0, 0, 0);
        }
        __syncthreads();
    }
    int col = n0 + wc * 32 + (lane & 31);
    if (col < N) {
        float bv = bf2f(bias[col]);
        for (int i = 0; i < 16; ++i) {
            int row = m0 + wr * 32 + (i & 3) + 8 * (i >> 2) + 4 * (lane >> 5);
            float v = acc[i] + bv;
            if (GELU) {
                float u = 0.7978845608028654f * (v + 0.044715f * v * v * v);
                v = 0.5f * v * (1.f + tanhf(u));
            }
            C[(size_t)row * N + col] = f2bf(v);
        }
    }
}

extern "C" void kernel_launch(void* const* d_in, const int* in_sizes, int n_in,
                              void* d_out, int out_size, void* d_ws, size_t ws_size,
                              hipStream_t stream) {
    const u16* x = (const u16*)d_in[0];
    const u16* w1 = (const u16*)d_in[1];
    const u16* b1 = (const u16*)d_in[2];
    const u16* w2 = (const u16*)d_in[3];
    const u16* b2 = (const u16*)d_in[4];
    u16* out = (u16*)d_out;
    u16* H = (u16*)d_ws;
    const int S = 2048, D = 512, F = 1376;
    gemm_simple<true><<<dim3((F + 63) / 64, S / 64), 256, 0, stream>>>(x, w1, b1, H, S, F, D);
    gemm_simple<false><<<dim3(D / 64, S / 64), 256, 0, stream>>>(H, w2, b2, out, S, D, F);
}
```

```cpp
#include <hip/hip_runtime.h>
#include <hip/hip_bf16.h>
#include <stdint.h>

typedef unsigned short u16;
typedef unsigned u32;
typedef unsigned long long u64;
typedef __attribute__((ext_vector_type(4))) unsigned u32x4;
typedef __attribute__((ext_vector_type(2))) unsigned u32x2;
typedef __attribute__((ext_vector_type(4))) __bf16 bf16x4;
typedef __attribute__((ext_vector_type(8))) __bf16 bf16x8;
typedef __attribute__((ext_vector_type(2))) __bf16 bf16x2;
typedef __attribute__((ext_vector_type(2))) float f32x2;
typedef __attribute__((ext_vector_type(16))) float f32x16;
typedef __amdgpu_buffer_rsrc_t rsrc_t;
#define LAS __attribute__((address_space(3)))
#define DI __device__ __forceinline__

constexpr int S_ = 2048, D_ = 512, F_ = 1376;
constexpr u32 SLOT = 28672, XTRA = 5 * SLOT;
constexpr u32 WS_ACK = 4096, WS_FLAG = 8192, WS_H = 1u << 20;
constexpr u32 H_BYTES = 64u * 86u * 1024u;
constexpr u32 SPIN_CAP = 1u << 17;

#define STAMP(i) do {} while (0)

DI u32 rfl(u32 x) { return __builtin_amdgcn_readfirstlane(x); }
DI void dma16(rsrc_t rs, u32 voff, u32 soff, u32 lds) {
    asm volatile("s_mov_b32 m0, %0\n\ts_nop 0\n\tbuffer_load_dwordx4 %1, %2, %3 offen lds" ::"s"(lds), "v"(voff), "s"(rs), "s"(soff) : "memory");
}
template <int N> DI void wait_bar() { asm volatile("s_waitcnt vmcnt(%0) lgkmcnt(0)\n\ts_barrier" ::"i"(N) : "memory"); }
DI bf16x4 trd(u32 a) { return __builtin_amdgcn_ds_read_tr16_b64_v4bf16((LAS bf16x4*)(size_t)a); }
DI bf16x8 cat(bf16x4 a, bf16x4 b) { return __builtin_shufflevector(a, b, 0, 1, 2, 3, 4, 5, 6, 7); }
DI bf16x8 rd128(u32 a) { return *(LAS bf16x8*)(size_t)a; }
#define MFMA(a, b, c) __builtin_amdgcn_mfma_f32_32x32x16_bf16((a), (b), (c), 0, 0, 0)

DI u32 gelu2(float a, float b) {
    f32x2 v = {a, b};
    f32x2 v2 = v * v;
    f32x2 t = v2 * (f32x2){-0.10294324f, -0.10294324f} + (f32x2){-2.30220820f, -2.30220820f};
    f32x2 z = t * v;
    f32x2 e = {__builtin_amdgcn_exp2f(z.x), __builtin_amdgcn_exp2f(z.y)};
    f32x2 d = e + (f32x2){1.f, 1.f};
    f32x2 r = {__builtin_amdgcn_rcpf(d.x), __builtin_amdgcn_rcpf(d.y)};
    f32x2 g = v * r;
    return __builtin_bit_cast(u32, __builtin_convertvector(g, bf16x2));
}
DI u32 pack2(float a, float b) { f32x2 g = {a, b}; return __builtin_bit_cast(u32, __builtin_convertvector(g, bf16x2)); }
DI float blo(u32 x) { return __uint_as_float(x << 16); }
DI float bhi(u32 x) { return __uint_as_float(x & 0xffff0000u); }

__global__ __launch_bounds__(256, 1) void mlp_fused(const u16* __restrict__ x, const u16* __restrict__ w1, const u16* __restrict__ b1,
                                                    const u16* __restrict__ w2, const u16* __restrict__ b2, u16* __restrict__ out,
                                                    char* __restrict__ ws) {
    STAMP(0);
    const u32 tid = threadIdx.x, lane = tid & 63;
    const u32 w = rfl(tid >> 6);
    const u32 b = blockIdx.x, xcd = b & 7, j = b >> 3;
    const u32 h = lane >> 5, blk = (lane >> 4) & 1, q_ = (lane & 15) >> 2, p_ = lane & 3, m_ = lane & 31;
    const u32 p1 = (xcd >> 1) * 4 + (j >> 3), tt = j & 7;
    const u32 t1 = (xcd & 1) ? 7 + tt : (tt == 7 ? 15 : tt);
    const u32 rot1 = (tt + 2 * (j >> 3)) & 7;
    const u32 r2 = xcd * 4 + (j >> 3), c2 = j & 7;
    const u32 mi = w >> 1, kj = w & 1;
    const u32 rho = (c2 * 5 + (r2 & 3) * 11) % 43;

    const rsrc_t rX = __builtin_amdgcn_make_buffer_rsrc((void*)x, 0, S_ * D_ * 2, 0x00020000);
    const rsrc_t rW1 = __builtin_amdgcn_make_buffer_rsrc((void*)w1, 0, D_ * F_ * 2, 0x00020000);
    const rsrc_t rW2 = __builtin_amdgcn_make_buffer_rsrc((void*)w2, 0, D_ * F_ * 2, 0x00020000);
    const rsrc_t rB1 = __builtin_amdgcn_make_buffer_rsrc((void*)b1, 0, F_ * 2, 0x00020000);
    const rsrc_t rB2 = __builtin_amdgcn_make_buffer_rsrc((void*)b2, 0, D_ * 2, 0x00020000);
    const rsrc_t rWS = __builtin_amdgcn_make_buffer_rsrc((void*)ws, 0, WS_H + H_BYTES, 0x00020000);
    const rsrc_t rO = __builtin_amdgcn_make_buffer_rsrc((void*)out, 0, S_ * D_ * 2, 0x00020000);

    u32x2 ev;
    { u32 z = 0; asm volatile("buffer_load_dwordx2 %0, %1, %2, 0 offen sc1" : "=v"(ev) : "v"(z), "s"(rWS) : "memory"); }

    const u32 w2v = (lane >> 3) * 1024 + (((lane & 7) ^ (((lane >> 4) & 1) << 2)) * 16);
    auto w2_soff = [&](u32 kjj, u32 i, u32 hs) -> u32 {
        u32 s = i + rho; if (s >= 43) s -= 43; s += 43 * kjj;
        return (16 * s + 8 * hs) * 1024 + c2 * 128;
    };
    auto w2_dma = [&](u32 d) {
        u32 q = d >> 1, kjj = q >= 40 ? 1 : 0, i = 3 + q - 40 * kjj;
        dma16(rW2, w2v, w2_soff(kjj, i, d & 1), d * 1024);
    };
    auto w2_pre = [&](u32 e) {
        u32 kjj = e >= 6 ? 1 : 0, ee = e - 6 * kjj;
        dma16(rW2, w2v, w2_soff(kjj, ee >> 1, e & 1), XTRA + e * 1024);
    };
    const u32 sw2 = (q_ >> 1) & 1;
    const u32 a2base = (4 * h + q_) * 128 + (2 * blk + (p_ >> 1)) * 16 + 8 * (p_ & 1);
    const u32 a2n0 = a2base + 64 * sw2, a2n1 = a2base + 64 * (sw2 ^ 1);
    bf16x8 pre[3][2];
    auto read_pre = [&]() {
#pragma unroll
        for (int i = 0; i < 3; ++i) {
            u32 L = XTRA + (kj * 3 + i) * 2048;
            pre[i][0] = cat(trd(a2n0 + L), trd(a2n0 + L + 1024));
            pre[i][1] = cat(trd(a2n1 + L), trd(a2n1 + L + 1024));
        }
    };
    u64 tag;
    auto make_tag = [&]() {
        u64 e = ((u64)rfl(ev.y) << 32) | rfl(ev.x);
        tag = e * 6364136223846793005ull + 1442695040888963407ull;
    };
    auto store_ack = [&]() {
        if (w == 0) {
            u32x2 tv = {(u32)tag, (u32)(tag >> 32)};
            u32 vo = WS_ACK + b * 8;
            if (lane == 0) asm volatile("buffer_store_dwordx2 %0, %1, %2, 0 offen sc1" ::"v"(tv), "v"(vo), "s"(rWS) : "memory");
        }
    };

    if (t1 != 15) {
        u32x2 bb[3][4];
#pragma unroll
        for (int u = 0; u < 3; ++u)
#pragma unroll
            for (int g = 0; g < 4; ++g) {
                u32 vo = (96 * t1 + 32 * u + 8 * g + 4 * h) * 2;
                asm volatile("buffer_load_dwordx2 %0, %1, %2, 0 offen" : "=v"(bb[u][g]) : "v"(vo), "s"(rB1) : "memory");
            }
        const u32 xr = lane >> 3, xc = (lane & 7) ^ (xr >> 1);
        const u32 xv0 = xr * 1024 + xc * 16, xv1 = xr * 1024 + (xc ^ 4) * 16;
        u32 wv[3];
#pragma unroll
        for (int i = 0; i < 3; ++i) { u32 p = i * 64 + lane; u32 row = w * 16 + p / 12, c = p % 12; wv[i] = row * 2752 + t1 * 192 + c * 16; }
        auto issue_chunk = [&](int ci) {
            u32 ch = (ci + rot1) & 7;
            u32 slot = (ci % 5) * SLOT;
            u32 xs = p1 * 131072 + w * 32768 + ch * 128;
            dma16(rX, xv0, xs, slot + w * 4096);
            dma16(rX, xv1, xs + 8192, slot + w * 4096 + 1024);
            dma16(rX, xv0, xs + 16384, slot + w * 4096 + 2048);
            dma16(rX, xv1, xs + 24576, slot + w * 4096 + 3072);
            u32 wsf = ch * 64 * 2752;
            dma16(rW1, wv[0], wsf, slot + 16384 + w * 3072);
            dma16(rW1, wv[1], wsf, slot + 16384 + w * 3072 + 1024);
            dma16(rW1, wv[2], wsf, slot + 16384 + w * 3072 + 2048);
        };
#pragma unroll
        for (int ci = 0; ci < 5; ++ci) issue_chunk(ci);
        STAMP(1);
        const u32 xa = (32 * w + m_) * 128 + ((h ^ ((m_ >> 1) & 7)) * 16);
        const u32 wa = 16384 + (8 * h + q_) * 192 + 32 * blk + 8 * p_;
        f32x16 acc[3];
        asm volatile("s_waitcnt vmcnt(35)" : "+v"(ev), "+v"(bb[0][0]), "+v"(bb[0][1]), "+v"(bb[0][2]), "+v"(bb[0][3]), "+v"(bb[1][0]), "+v"(bb[1][1]),
                     "+v"(bb[1][2]), "+v"(bb[1][3]), "+v"(bb[2][0]), "+v"(bb[2][1]), "+v"(bb[2][2]), "+v"(bb[2][3])::"memory");
#pragma unroll
        for (int u = 0; u < 3; ++u)
#pragma unroll
            for (int g = 0; g < 4; ++g) {
                acc[u][4 * g + 0] = blo(bb[u][g].x); acc[u][4 * g + 1] = bhi(bb[u][g].x);
                acc[u][4 * g + 2] = blo(bb[u][g].y); acc[u][4 * g + 3] = bhi(bb[u][g].y);
            }
        make_tag();
        store_ack();
        auto compute_chunk = [&](int ci) {
            u32 slot = (ci % 5) * SLOT;
#pragma unroll
            for (int s = 0; s < 4; ++s) {
                bf16x8 B = rd128((xa ^ (s << 5)) + slot);
#pragma unroll
                for (int u = 0; u < 3; ++u) {
                    u32 a = wa + slot + s * 3072 + u * 64;
                    bf16x8 A = cat(trd(a), trd(a + 768));
                    acc[u] = MFMA(A, B, acc[u]);
                }
            }
        };
        auto w2_job = [&](int d0, int n) {
            for (int i = 0; i < n; ++i) w2_dma(d0 + w * n + i);
        };
        wait_bar<28>(); STAMP(2);
        for (int e = 0; e < 3; ++e) w2_pre(w * 3 + e);
        compute_chunk(0);
        wait_bar<21>(); issue_chunk(5); compute_chunk(1);
        wait_bar<21>(); issue_chunk(6); compute_chunk(2);
        wait_bar<21>(); issue_chunk(7); compute_chunk(3);
        wait_bar<21>(); w2_job(84, 7); compute_chunk(4);
        wait_bar<21>(); w2_job(112, 7); compute_chunk(5);
        read_pre();
        wait_bar<21>(); w2_job(0, 7); compute_chunk(6);
        wait_bar<21>(); w2_job(28, 7); STAMP(3); compute_chunk(7);
        wait_bar<63>(); w2_job(56, 7); w2_job(140, 5);
        STAMP(4);
        const u32 mt = p1 * 4 + w;
        const u32 nun = (t1 == 14) ? 1 : 3;
#pragma unroll
        for (int u = 0; u < 3; ++u) {
            if (u < (int)nun) {
#pragma unroll
                for (int s = 0; s < 2; ++s) {
                    u32x4 o;
                    o.x = gelu2(acc[u][8 * s + 0], acc[u][8 * s + 1]);
                    o.y = gelu2(acc[u][8 * s + 2], acc[u][8 * s + 3]);
                    o.z = gelu2(acc[u][8 * s + 4], acc[u][8 * s + 5]);
                    o.w = gelu2(acc[u][8 * s + 6], acc[u][8 * s + 7]);
                    u32 so = WS_H + (mt * 86 + 2 * (3 * t1 + u) + s) * 1024;
                    u32 vo = lane * 16;
                    asm volatile("buffer_store_dwordx4 %0, %1, %2, %3 offen sc1\n\ts_nop 1" ::"v"(o), "v"(vo), "s"(rWS), "s"(so) : "memory");
                }
            }
        }
        STAMP(5);
        asm volatile("s_waitcnt vmcnt(0)" ::: "memory");
        {
            u32x2 tv = {(u32)tag, (u32)(tag >> 32)};
            u32 vo = WS_FLAG + (mt * 16 + t1) * 128;
            if (lane == 0) asm volatile("buffer_store_dwordx2 %0, %1, %2, 0 offen sc1" ::"v"(tv), "v"(vo), "s"(rWS) : "memory");
        }
        STAMP(6);
    } else {
        for (int e = 0; e < 3; ++e) w2_pre(w * 3 + e);
        asm volatile("s_waitcnt vmcnt(0)" : "+v"(ev)::"memory");
        make_tag();
        store_ack();
        wait_bar<63>();
        read_pre();
        wait_bar<63>();
        for (int i = 0; i < 40; ++i) w2_dma(w * 40 + i);
        if (p1 == 0 && w == 3) {
            bool ok = false;
            for (u32 spin = 0; spin < SPIN_CAP && !ok; ++spin) {
                u32x4 a0, a1; u32 vo = WS_ACK + lane * 32;
                asm volatile("buffer_load_dwordx4 %0, %2, %3, 0 offen sc1\n\tbuffer_load_dwordx4 %1, %2, %3, 0 offen offset:16 sc1\n\ts_waitcnt vmcnt(0)"
                             : "=&v"(a0), "=&v"(a1) : "v"(vo), "s"(rWS) : "memory");
                u32 lo = (u32)tag, hi = (u32)(tag >> 32);
                bool m = a0.x == lo && a0.y == hi && a0.z == lo && a0.w == hi && a1.x == lo && a1.y == hi && a1.z == lo && a1.w == hi;
                ok = __all(m);
                if (!ok) __builtin_amdgcn_s_sleep(8);
            }
            if (ok) {
                u32x2 tv = {(u32)tag, (u32)(tag >> 32)}; u32 vo = 0;
                if (lane == 0) asm volatile("buffer_store_dwordx2 %0, %1, %2, 0 offen sc1" ::"v"(tv), "v"(vo), "s"(rWS) : "memory");
            }
        }
        asm volatile("s_waitcnt vmcnt(0)" ::: "memory");
    }
    u32x2 b2v[4];
#pragma unroll
    for (int g = 0; g < 4; ++g) {
        u32 vo = (64 * c2 + 32 * kj + 8 * g + 4 * h) * 2;
        asm volatile("buffer_load_dwordx2 %0, %1, %2, 0 offen" : "=v"(b2v[g]) : "v"(vo), "s"(rB2) : "memory");
    }
    wait_bar<63>();
    const u32 mt2 = r2 * 2 + mi;
    {
        u32 vo = WS_FLAG + (mt2 * 16 + (kj ? 7 : 0) + (lane & 7)) * 128;
        u32 lo = (u32)tag, hi = (u32)(tag >> 32);
        for (u32 spin = 0; spin < SPIN_CAP; ++spin) {
            u32x2 f;
            asm volatile("buffer_load_dwordx2 %0, %1, %2, 0 offen sc1\n\ts_waitcnt vmcnt(0)" : "=&v"(f) : "v"(vo), "s"(rWS) : "memory");
            if (__all(f.x == lo && f.y == hi)) break;
            __builtin_amdgcn_s_sleep(1);
        }
    }
    STAMP(7);
    u32x4 hf[43];
    {
        u32 vo = lane * 16;
        u32 base = WS_H + (mt2 * 86 + 43 * kj) * 1024;
        u32 s = rho;
#pragma unroll
        for (int i = 0; i < 43; ++i) {
            u32 so = base + s * 1024;
            asm volatile("buffer_load_dwordx4 %0, %1, %2, %3 offen sc1" : "=v"(hf[i]) : "v"(vo), "s"(rWS), "s"(so) : "memory");
            s = (s == 42) ? 0 : s + 1;
        }
    }
    f32x16 o0, o1;
    asm volatile("" : "+v"(b2v[0]), "+v"(b2v[1]), "+v"(b2v[2]), "+v"(b2v[3]));
    {
        f32x16 bi;
#pragma unroll
        for (int g = 0; g < 4; ++g) { bi[4 * g] = blo(b2v[g].x); bi[4 * g + 1] = bhi(b2v[g].x); bi[4 * g + 2] = blo(b2v[g].y); bi[4 * g + 3] = bhi(b2v[g].y); }
#pragma unroll
        for (int i = 0; i < 16; ++i) { o0[i] = kj ? 0.f : bi[i]; o1[i] = kj ? bi[i] : 0.f; }
    }
#define STEP(i)                                                                                          \
    {                                                                                                    \
        bf16x8 A0, A1;                                                                                   \
        if (i < 3) { A0 = pre[i < 3 ? i : 0][0]; A1 = pre[i < 3 ? i : 0][1]; }                            \
        else {                                                                                           \
            u32 L = (kj * 40 + (i - 3)) * 2048;                                                          \
            A0 = cat(trd(a2n0 + L), trd(a2n0 + L + 1024));                                               \
            A1 = cat(trd(a2n1 + L), trd(a2n1 + L + 1024));                                               \
        }                                                                                                \
        asm volatile("s_waitcnt vmcnt(%1)" : "+v"(hf[i]) : "i"(42 - i));                                 \
        bf16x8 B = __builtin_bit_cast(bf16x8, hf[i]);                                                    \
        o0 = MFMA(A0, B, o0);                                                                            \
        o1 = MFMA(A1, B, o1);                                                                            \
    }
    STEP(0) STEP(1) STEP(2) STEP(3) STEP(4) STEP(5) STEP(6) STEP(7) STEP(8) STEP(9)
    STEP(10) STEP(11) STEP(12) STEP(13) STEP(14) STEP(15) STEP(16) STEP(17) STEP(18) STEP(19)
    STEP(20) STEP(21) STEP(22) STEP(23) STEP(24) STEP(25) STEP(26) STEP(27) STEP(28) STEP(29)
    STEP(30) STEP(31) STEP(32) STEP(33) STEP(34) STEP(35) STEP(36) STEP(37) STEP(38) STEP(39)
    STEP(40) STEP(41) STEP(42)
    STAMP(8);
    wait_bar<63>();
    {
        f32x16 snd = kj ? o0 : o1;
        u32 a = w * 4096 + lane * 16;
#pragma unroll
        for (int g = 0; g < 4; ++g) {
            typedef __attribute__((ext_vector_type(4))) float f32x4;
            f32x4 v = {snd[4 * g], snd[4 * g + 1], snd[4 * g + 2], snd[4 * g + 3]};
            *(LAS f32x4*)(size_t)(a + g * 1024) = v;
        }
    }
    wait_bar<63>();
    {
        f32x16 kp = kj ? o1 : o0;
        u32 a = (w ^ 1) * 4096 + lane * 16;
        u32 d[8];
#pragma unroll
        for (int g = 0; g < 4; ++g) {
            typedef __attribute__((ext_vector_type(4))) float f32x4;
            f32x4 v = *(LAS f32x4*)(size_t)(a + g * 1024);
            d[2 * g] = pack2(kp[4 * g] + v[0], kp[4 * g + 1] + v[1]);
            d[2 * g + 1] = pack2(kp[4 * g + 2] + v[2], kp[4 * g + 3] + v[3]);
        }
        u32 row = r2 * 64 + mi * 32 + m_;
        u32 vo = row * 1024 + (c2 * 64 + kj * 32) * 2 + h * 16;
#pragma unroll
        for (int k = 0; k < 2; ++k) {
            auto r0 = __builtin_amdgcn_permlane32_swap(d[4 * k], d[4 * k + 2], false, false);
            auto r1 = __builtin_amdgcn_permlane32_swap(d[4 * k + 1], d[4 * k + 3], false, false);
            u32x4 o = {r0[0], r1[0], r0[1], r1[1]};
            u32 so = k * 32;
            asm volatile("buffer_store_dwordx4 %0, %1, %2, %3 offen\n\ts_nop 1" ::"v"(o), "v"(vo), "s"(rO), "s"(so) : "memory");
        }
    }
    STAMP(9);
}

extern "C" void kernel_launch(void* const* d_in, const int* in_sizes, int n_in, void* d_out, int out_size, void* d_ws, size_t ws_size,
                              hipStream_t stream) {
    (void)in_sizes; (void)n_in; (void)out_size; (void)ws_size;
    hipFuncSetAttribute((const void*)mlp_fused, hipFuncAttributeMaxDynamicSharedMemorySize, 163840);
    mlp_fused<<<256, 256, 163840, stream>>>((const u16*)d_in[0], (const u16*)d_in[1], (const u16*)d_in[2], (const u16*)d_in[3],
                                            (const u16*)d_in[4], (u16*)d_out, (char*)d_ws);
}
```

```cpp
#define CODEPF 1
#include <hip/hip_runtime.h>
#include <hip/hip_bf16.h>
#include <stdint.h>

typedef unsigned short u16;
typedef unsigned u32;
typedef unsigned long long u64;
typedef __attribute__((ext_vector_type(4))) unsigned u32x4;
typedef __attribute__((ext_vector_type(2))) unsigned u32x2;
typedef __attribute__((ext_vector_type(4))) __bf16 bf16x4;
typedef __attribute__((ext_vector_type(8))) __bf16 bf16x8;
typedef __attribute__((ext_vector_type(2))) __bf16 bf16x2;
typedef __attribute__((ext_vector_type(2))) float f32x2;
typedef __attribute__((ext_vector_type(16))) float f32x16;
typedef __amdgpu_buffer_rsrc_t rsrc_t;
#define LAS __attribute__((address_space(3)))
#define DI __device__ __forceinline__

constexpr int S_ = 2048, D_ = 512, F_ = 1376;
constexpr u32 SLOT = 28672, XTRA = 5 * SLOT;
constexpr u32 WS_ACK = 4096, WS_FLAG = 8192, WS_H = 1u << 20;
constexpr u32 H_BYTES = 64u * 86u * 1024u;
constexpr u32 SPIN_CAP = 1u << 17;

#define STAMP(i) do {} while (0)

DI u32 rfl(u32 x) { return __builtin_amdgcn_readfirstlane(x); }
DI void dma16(rsrc_t rs, u32 voff, u32 soff, u32 lds) {
    asm volatile("s_mov_b32 m0, %0\n\ts_nop 0\n\tbuffer_load_dwordx4 %1, %2, %3 offen lds" ::"s"(lds), "v"(voff), "s"(rs), "s"(soff) : "memory");
}
template <int N> DI void wait_bar() { asm volatile("s_waitcnt vmcnt(%0) lgkmcnt(0)\n\ts_barrier" ::"i"(N) : "memory"); }
DI bf16x4 trd(u32 a) { return __builtin_amdgcn_ds_read_tr16_b64_v4bf16((LAS bf16x4*)(size_t)a); }
DI bf16x8 cat(bf16x4 a, bf16x4 b) { return __builtin_shufflevector(a, b, 0, 1, 2, 3, 4, 5, 6, 7); }
DI bf16x8 rd128(u32 a) { return *(LAS bf16x8*)(size_t)a; }
#define MFMA(a, b, c) __builtin_amdgcn_mfma_f32_32x32x16_bf16((a), (b), (c), 0, 0, 0)

DI u32 gelu2(float a, float b) {
    f32x2 v = {a, b};
    f32x2 v2 = v * v;
    f32x2 t = v2 * (f32x2){-0.10294324f, -0.10294324f} + (f32x2){-2.30220820f, -2.30220820f};
    f32x2 z = t * v;
    f32x2 e = {__builtin_amdgcn_exp2f(z.x), __builtin_amdgcn_exp2f(z.y)};
    f32x2 d = e + (f32x2){1.f, 1.f};
    f32x2 r = {__builtin_amdgcn_rcpf(d.x), __builtin_amdgcn_rcpf(d.y)};
    f32x2 g = v * r;
    return __builtin_bit_cast(u32, __builtin_convertvector(g, bf16x2));
}
DI u32 pack2(float a, float b) { f32x2 g = {a, b}; return __builtin_bit_cast(u32, __builtin_convertvector(g, bf16x2)); }
DI float blo(u32 x) { return __uint_as_float(x << 16); }
DI float bhi(u32 x) { return __uint_as_float(x & 0xffff0000u); }

__global__ __launch_bounds__(256, 1) void mlp_fused(const u16* __restrict__ x, const u16* __restrict__ w1, const u16* __restrict__ b1,
                                                    const u16* __restrict__ w2, const u16* __restrict__ b2, u16* __restrict__ out,
                                                    char* __restrict__ ws) {
    STAMP(0);
    const u32 tid = threadIdx.x, lane = tid & 63;
    const u32 w = rfl(tid >> 6);
    u32 pfd;
    {
        u64 pc; asm volatile("s_getpc_b64 %0" : "=s"(pc));
        pc &= ~255ull;
        u32 vo = min(tid * 128u, 20480u);
        asm volatile("global_load_dword %0, %1, %2" : "=v"(pfd) : "v"(vo), "s"(pc) : "memory");
    }
    const u32 b = blockIdx.x, xcd = b & 7, j = b >> 3;
    const u32 h = lane >> 5, blk = (lane >> 4) & 1, q_ = (lane & 15) >> 2, p_ = lane & 3, m_ = lane & 31;
    const u32 p1 = (xcd >> 1) * 4 + (j >> 3), tt = j & 7;
    const u32 t1 = (xcd & 1) ? 7 + tt : (tt == 7 ? 15 : tt);
    const u32 rot1 = (tt + 2 * (j >> 3)) & 7;
    const u32 r2 = xcd * 4 + (j >> 3), c2 = j & 7;
    const u32 mi = w >> 1, kj = w & 1;
    const u32 rho = (c2 * 5 + (r2 & 3) * 11) % 43;

    const rsrc_t rX = __builtin_amdgcn_make_buffer_rsrc((void*)x, 0, S_ * D_ * 2, 0x00020000);
    const rsrc_t rW1 = __builtin_amdgcn_make_buffer_rsrc((void*)w1, 0, D_ * F_ * 2, 0x00020000);
    const rsrc_t rW2 = __builtin_amdgcn_make_buffer_rsrc((void*)w2, 0, D_ * F_ * 2, 0x00020000);
    const rsrc_t rB1 = __builtin_amdgcn_make_buffer_rsrc((void*)b1, 0, F_ * 2, 0x00020000);
    const rsrc_t rB2 = __builtin_amdgcn_make_buffer_rsrc((void*)b2, 0, D_ * 2, 0x00020000);
    const rsrc_t rWS = __builtin_amdgcn_make_buffer_rsrc((void*)ws, 0, WS_H + H_BYTES, 0x00020000);
    const rsrc_t rO = __builtin_amdgcn_make_buffer_rsrc((void*)out, 0, S_ * D_ * 2, 0x00020000);

    u32x2 ev;
    { u32 z = 0; asm volatile("buffer_load_dwordx2 %0, %1, %2, 0 offen sc1" : "=v"(ev) : "v"(z), "s"(rWS) : "memory"); }

    const u32 w2v = (lane >> 3) * 1024 + (((lane & 7) ^ (((lane >> 4) & 1) << 2)) * 16);
    auto w2_soff = [&](u32 kjj, u32 i, u32 hs) -> u32 {
        u32 s = i + rho; if (s >= 43) s -= 43; s += 43 * kjj;
        return (16 * s + 8 * hs) * 1024 + c2 * 128;
    };
    auto w2_dma = [&](u32 d) {
        u32 q = d >> 1, kjj = q >= 40 ? 1 : 0, i = 3 + q - 40 * kjj;
        dma16(rW2, w2v, w2_soff(kjj, i, d & 1), d * 1024);
    };
    auto w2_pre = [&](u32 e) {
        u32 kjj = e >= 6 ? 1 : 0, ee = e - 6 * kjj;
        dma16(rW2, w2v, w2_soff(kjj, ee >> 1, e & 1), XTRA + e * 1024);
    };
    const u32 sw2 = (q_ >> 1) & 1;
    const u32 a2base = (4 * h + q_) * 128 + (2 * blk + (p_ >> 1)) * 16 + 8 * (p_ & 1);
    const u32 a2n0 = a2base + 64 * sw2, a2n1 = a2base + 64 * (sw2 ^ 1);
    bf16x8 pre[3][2];
    auto read_pre = [&]() {
#pragma unroll
        for (int i = 0; i < 3; ++i) {
            u32 L = XTRA + (kj * 3 + i) * 2048;
            pre[i][0] = cat(trd(a2n0 + L), trd(a2n0 + L + 1024));
            pre[i][1] = cat(trd(a2n1 + L), trd(a2n1 + L + 1024));
        }
    };
    u64 tag;
    auto make_tag = [&]() {
        u64 e = ((u64)rfl(ev.y) << 32) | rfl(ev.x);
        tag = e * 6364136223846793005ull + 1442695040888963407ull;
    };
    auto store_ack = [&]() {
        if (w == 0) {
            u32x2 tv = {(u32)tag, (u32)(tag >> 32)};
            u32 vo = WS_ACK + b * 8;
            if (lane == 0) asm volatile("buffer_store_dwordx2 %0, %1, %2, 0 offen sc1" ::"v"(tv), "v"(vo), "s"(rWS) : "memory");
        }
    };

    if (t1 != 15) {
        u32x2 bb[3][4];
#pragma unroll
        for (int u = 0; u < 3; ++u)
#pragma unroll
            for (int g = 0; g < 4; ++g) {
                u32 vo = (96 * t1 + 32 * u + 8 * g + 4 * h) * 2;
                asm volatile("buffer_load_dwordx2 %0, %1, %2, 0 offen" : "=v"(bb[u][g]) : "v"(vo), "s"(rB1) : "memory");
            }
        const u32 xr = lane >> 3, xc = (lane & 7) ^ (xr >> 1);
        const u32 xv0 = xr * 1024 + xc * 16, xv1 = xr * 1024 + (xc ^ 4) * 16;
        u32 wv[3];
#pragma unroll
        for (int i = 0; i < 3; ++i) { u32 p = i * 64 + lane; u32 row = w * 16 + p / 12, c = p % 12; wv[i] = row * 2752 + t1 * 192 + c * 16; }
        auto issue_chunk = [&](int ci) {
            u32 ch = (ci + rot1) & 7;
            u32 slot = (ci % 5) * SLOT;
            u32 xs = p1 * 131072 + w * 32768 + ch * 128;
            dma16(rX, xv0, xs, slot + w * 4096);
            dma16(rX, xv1, xs + 8192, slot + w * 4096 + 1024);
            dma16(rX, xv0, xs + 16384, slot + w * 4096 + 2048);
            dma16(rX, xv1, xs + 24576, slot + w * 4096 + 3072);
            u32 wsf = ch * 64 * 2752;
            dma16(rW1, wv[0], wsf, slot + 16384 + w * 3072);
            dma16(rW1, wv[1], wsf, slot + 16384 + w * 3072 + 1024);
            dma16(rW1, wv[2], wsf, slot + 16384 + w * 3072 + 2048);
        };
#pragma unroll
        for (int ci = 0; ci < 5; ++ci) issue_chunk(ci);
        STAMP(1);
        const u32 xa = (32 * w + m_) * 128 + ((h ^ ((m_ >> 1) & 7)) * 16);
        const u32 wa = 16384 + (8 * h + q_) * 192 + 32 * blk + 8 * p_;
        f32x16 acc[3];
        asm volatile("s_waitcnt vmcnt(35)" : "+v"(ev), "+v"(bb[0][0]), "+v"(bb[0][1]), "+v"(bb[0][2]), "+v"(bb[0][3]), "+v"(bb[1][0]), "+v"(bb[1][1]),
                     "+v"(bb[1][2]), "+v"(bb[1][3]), "+v"(bb[2][0]), "+v"(bb[2][1]), "+v"(bb[2][2]), "+v"(bb[2][3])::"memory");
#pragma unroll
        for (int u = 0; u < 3; ++u)
#pragma unroll
            for (int g = 0; g < 4; ++g) {
                acc[u][4 * g + 0] = blo(bb[u][g].x); acc[u][4 * g + 1] = bhi(bb[u][g].x);
                acc[u][4 * g + 2] = blo(bb[u][g].y); acc[u][4 * g + 3] = bhi(bb[u][g].y);
            }
        asm volatile("" :: "v"(pfd));
        make_tag();
        store_ack();
        auto compute_chunk = [&](int ci) {
            u32 slot = (ci % 5) * SLOT;
#pragma unroll
            for (int s = 0; s < 4; ++s) {
                bf16x8 B = rd128((xa ^ (s << 5)) + slot);
#pragma unroll
                for (int u = 0; u < 3; ++u) {
                    u32 a = wa + slot + s * 3072 + u * 64;
                    bf16x8 A = cat(trd(a), trd(a + 768));
                    acc[u] = MFMA(A, B, acc[u]);
                }
            }
        };
        auto w2_job = [&](int d0, int n) {
            for (int i = 0; i < n; ++i) w2_dma(d0 + w * n + i);
        };
        wait_bar<28>(); STAMP(2);
        for (int e = 0; e < 3; ++e) w2_pre(w * 3 + e);
        compute_chunk(0);
        wait_bar<21>(); issue_chunk(5); compute_chunk(1);
        wait_bar<21>(); issue_chunk(6); compute_chunk(2);
        wait_bar<21>(); issue_chunk(7); compute_chunk(3);
        wait_bar<21>(); w2_job(84, 7); compute_chunk(4);
        wait_bar<21>(); w2_job(112, 7); compute_chunk(5);
        read_pre();
        wait_bar<21>(); w2_job(0, 7); compute_chunk(6);
        wait_bar<21>(); w2_job(28, 7); STAMP(3); compute_chunk(7);
        wait_bar<63>(); w2_job(56, 7); w2_job(140, 5);
        STAMP(4);
        const u32 mt = p1 * 4 + w;
        const u32 nun = (t1 == 14) ? 1 : 3;
#pragma unroll
        for (int u = 0; u < 3; ++u) {
            if (u < (int)nun) {
#pragma unroll
                for (int s = 0; s < 2; ++s) {
                    u32x4 o;
                    o.x = gelu2(acc[u][8 * s + 0], acc[u][8 * s + 1]);
                    o.y = gelu2(acc[u][8 * s + 2], acc[u][8 * s + 3]);
                    o.z = gelu2(acc[u][8 * s + 4], acc[u][8 * s + 5]);
                    o.w = gelu2(acc[u][8 * s + 6], acc[u][8 * s + 7]);
                    u32 so = WS_H + (mt * 86 + 2 * (3 * t1 + u) + s) * 1024;
                    u32 vo = lane * 16;
                    asm volatile("buffer_store_dwordx4 %0, %1, %2, %3 offen sc1\n\ts_nop 1" ::"v"(o), "v"(vo), "s"(rWS), "s"(so) : "memory");
                }
            }
        }
        STAMP(5);
        asm volatile("s_waitcnt vmcnt(0)" ::: "memory");
        {
            u32x2 tv = {(u32)tag, (u32)(tag >> 32)};
            u32 vo = WS_FLAG + (mt * 16 + t1) * 128;
            if (lane == 0) asm volatile("buffer_store_dwordx2 %0, %1, %2, 0 offen sc1" ::"v"(tv), "v"(vo), "s"(rWS) : "memory");
        }
        STAMP(6);
    } else {
        for (int e = 0; e < 3; ++e) w2_pre(w * 3 + e);
        asm volatile("s_waitcnt vmcnt(0)" : "+v"(ev)::"memory");
        asm volatile("" :: "v"(pfd));
        make_tag();
        store_ack();
        wait_bar<63>();
        read_pre();
        wait_bar<63>();
        for (int i = 0; i < 40; ++i) w2_dma(w * 40 + i);
        if (p1 == 0 && w == 3) {
            bool ok = false;
            for (u32 spin = 0; spin < SPIN_CAP && !ok; ++spin) {
                u32x4 a0, a1; u32 vo = WS_ACK + lane * 32;
                asm volatile("buffer_load_dwordx4 %0, %2, %3, 0 offen sc1\n\tbuffer_load_dwordx4 %1, %2, %3, 0 offen offset:16 sc1\n\ts_waitcnt vmcnt(0)"
                             : "=&v"(a0), "=&v"(a1) : "v"(vo), "s"(rWS) : "memory");
                u32 lo = (u32)tag, hi = (u32)(tag >> 32);
                bool m = a0.x == lo && a0.y == hi && a0.z == lo && a0.w == hi && a1.x == lo && a1.y == hi && a1.z == lo && a1.w == hi;
                ok = __all(m);
                if (!ok) __builtin_amdgcn_s_sleep(8);
            }
            if (ok) {
                u32x2 tv = {(u32)tag, (u32)(tag >> 32)}; u32 vo = 0;
                if (lane == 0) asm volatile("buffer_store_dwordx2 %0, %1, %2, 0 offen sc1" ::"v"(tv), "v"(vo), "s"(rWS) : "memory");
            }
        }
        asm volatile("s_waitcnt vmcnt(0)" ::: "memory");
    }
    u32x2 b2v[4];
#pragma unroll
    for (int g = 0; g < 4; ++g) {
        u32 vo = (64 * c2 + 32 * kj + 8 * g + 4 * h) * 2;
        asm volatile("buffer_load_dwordx2 %0, %1, %2, 0 offen" : "=v"(b2v[g]) : "v"(vo), "s"(rB2) : "memory");
    }
    wait_bar<63>();
    const u32 mt2 = r2 * 2 + mi;
    {
        u32 vo = WS_FLAG + (mt2 * 16 + (kj ? 7 : 0) + (lane & 7)) * 128;
        u32 lo = (u32)tag, hi = (u32)(tag >> 32);
        for (u32 spin = 0; spin < SPIN_CAP; ++spin) {
            u32x2 f;
            asm volatile("buffer_load_dwordx2 %0, %1, %2, 0 offen sc1\n\ts_waitcnt vmcnt(0)" : "=&v"(f) : "v"(vo), "s"(rWS) : "memory");
            if (__all(f.x == lo && f.y == hi)) break;
            __builtin_amdgcn_s_sleep(1);
        }
    }
    STAMP(7);
    u32x4 hf[43];
    {
        u32 vo = lane * 16;
        u32 base = WS_H + (mt2 * 86 + 43 * kj) * 1024;
        u32 s = rho;
#pragma unroll
        for (int i = 0; i < 43; ++i) {
            u32 so = base + s * 1024;
            asm volatile("buffer_load_dwordx4 %0, %1, %2, %3 offen sc1" : "=v"(hf[i]) : "v"(vo), "s"(rWS), "s"(so) : "memory");
            s = (s == 42) ? 0 : s + 1;
        }
    }
    f32x16 o0, o1;
    asm volatile("" : "+v"(b2v[0]), "+v"(b2v[1]), "+v"(b2v[2]), "+v"(b2v[3]));
    {
        f32x16 bi;
#pragma unroll
        for (int g = 0; g < 4; ++g) { bi[4 * g] = blo(b2v[g].x); bi[4 * g + 1] = bhi(b2v[g].x); bi[4 * g + 2] = blo(b2v[g].y); bi[4 * g + 3] = bhi(b2v[g].y); }
#pragma unroll
        for (int i = 0; i < 16; ++i) { o0[i] = kj ? 0.f : bi[i]; o1[i] = kj ? bi[i] : 0.f; }
    }
#define STEP(i)                                                                                          \
    {                                                                                                    \
        bf16x8 A0, A1;                                                                                   \
        if (i < 3) { A0 = pre[i < 3 ? i : 0][0]; A1 = pre[i < 3 ? i : 0][1]; }                            \
        else {                                                                                           \
            u32 L = (kj * 40 + (i - 3)) * 2048;                                                          \
            A0 = cat(trd(a2n0 + L), trd(a2n0 + L + 1024));                                               \
            A1 = cat(trd(a2n1 + L), trd(a2n1 + L + 1024));                                               \
        }                                                                                                \
        asm volatile("s_waitcnt vmcnt(%1)" : "+v"(hf[i]) : "i"(42 - i));                                 \
        bf16x8 B = __builtin_bit_cast(bf16x8, hf[i]);                                                    \
        o0 = MFMA(A0, B, o0);                                                                            \
        o1 = MFMA(A1, B, o1);                                                                            \
    }
    STEP(0) STEP(1) STEP(2) STEP(3) STEP(4) STEP(5) STEP(6) STEP(7) STEP(8) STEP(9)
    STEP(10) STEP(11) STEP(12) STEP(13) STEP(14) STEP(15) STEP(16) STEP(17) STEP(18) STEP(19)
    STEP(20) STEP(21) STEP(22) STEP(23) STEP(24) STEP(25) STEP(26) STEP(27) STEP(28) STEP(29)
    STEP(30) STEP(31) STEP(32) STEP(33) STEP(34) STEP(35) STEP(36) STEP(37) STEP(38) STEP(39)
    STEP(40) STEP(41) STEP(42)
    STAMP(8);
    wait_bar<63>();
    {
        f32x16 snd = kj ? o0 : o1;
        u32 a = w * 4096 + lane * 16;
#pragma unroll
        for (int g = 0; g < 4; ++g) {
            typedef __attribute__((ext_vector_type(4))) float f32x4;
            f32x4 v = {snd[4 * g], snd[4 * g + 1], snd[4 * g + 2], snd[4 * g + 3]};
            *(LAS f32x4*)(size_t)(a + g * 1024) = v;
        }
    }
    wait_bar<63>();
    {
        f32x16 kp = kj ? o1 : o0;
        u32 a = (w ^ 1) * 4096 + lane * 16;
        u32 d[8];
#pragma unroll
        for (int g = 0; g < 4; ++g) {
            typedef __attribute__((ext_vector_type(4))) float f32x4;
            f32x4 v = *(LAS f32x4*)(size_t)(a + g * 1024);
            d[2 * g] = pack2(kp[4 * g] + v[0], kp[4 * g + 1] + v[1]);
            d[2 * g + 1] = pack2(kp[4 * g + 2] + v[2], kp[4 * g + 3] + v[3]);
        }
        u32 row = r2 * 64 + mi * 32 + m_;
        u32 vo = row * 1024 + (c2 * 64 + kj * 32) * 2 + h * 16;
#pragma unroll
        for (int k = 0; k < 2; ++k) {
            auto r0 = __builtin_amdgcn_permlane32_swap(d[4 * k], d[4 * k + 2], false, false);
            auto r1 = __builtin_amdgcn_permlane32_swap(d[4 * k + 1], d[4 * k + 3], false, false);
            u32x4 o = {r0[0], r1[0], r0[1], r1[1]};
            u32 so = k * 32;
            asm volatile("buffer_store_dwordx4 %0, %1, %2, %3 offen\n\ts_nop 1" ::"v"(o), "v"(vo), "s"(rO), "s"(so) : "memory");
        }
    }
    STAMP(9);
}

extern "C" void kernel_launch(void* const* d_in, const int* in_sizes, int n_in, void* d_out, int out_size, void* d_ws, size_t ws_size,
                              hipStream_t stream) {
    (void)in_sizes; (void)n_in; (void)out_size; (void)ws_size;
    hipFuncSetAttribute((const void*)mlp_fused, hipFuncAttributeMaxDynamicSharedMemorySize, 163840);
    mlp_fused<<<256, 256, 163840, stream>>>((const u16*)d_in[0], (const u16*)d_in[1], (const u16*)d_in[2], (const u16*)d_in[3],
                                            (const u16*)d_in[4], (u16*)d_out, (char*)d_ws);
}
```

```cpp
#define PROBE_FROM 0
#define PROBE_TO 10
#define CODEPF 1
#include <hip/hip_runtime.h>
#include <hip/hip_bf16.h>
#include <stdint.h>

typedef unsigned short u16;
typedef unsigned u32;
typedef unsigned long long u64;
typedef __attribute__((ext_vector_type(4))) unsigned u32x4;
typedef __attribute__((ext_vector_type(2))) unsigned u32x2;
typedef __attribute__((ext_vector_type(4))) __bf16 bf16x4;
typedef __attribute__((ext_vector_type(8))) __bf16 bf16x8;
typedef __attribute__((ext_vector_type(2))) __bf16 bf16x2;
typedef __attribute__((ext_vector_type(2))) float f32x2;
typedef __attribute__((ext_vector_type(16))) float f32x16;
typedef __amdgpu_buffer_rsrc_t rsrc_t;
#define LAS __attribute__((address_space(3)))
#define DI __device__ __forceinline__

constexpr int S_ = 2048, D_ = 512, F_ = 1376;
constexpr u32 SLOT = 28672, XTRA = 5 * SLOT;
constexpr u32 WS_ACK = 4096, WS_FLAG = 8192, WS_H = 1u << 20;
constexpr u32 H_BYTES = 64u * 86u * 1024u;
constexpr u32 SPIN_CAP = 1u << 17;

#define STAMP(i) do { if (threadIdx.x == 0) { u64 t_; asm volatile("s_memrealtime %0\n\ts_waitcnt lgkmcnt(0)" : "=s"(t_) :: "memory"); stp[i] = t_; } } while (0)

DI u32 rfl(u32 x) { return __builtin_amdgcn_readfirstlane(x); }
DI void dma16(rsrc_t rs, u32 voff, u32 soff, u32 lds) {
    asm volatile("s_mov_b32 m0, %0\n\ts_nop 0\n\tbuffer_load_dwordx4 %1, %2, %3 offen lds" ::"s"(lds), "v"(voff), "s"(rs), "s"(soff) : "memory");
}
template <int N> DI void wait_bar() { asm volatile("s_waitcnt vmcnt(%0) lgkmcnt(0)\n\ts_barrier" ::"i"(N) : "memory"); }
DI bf16x4 trd(u32 a) { return __builtin_amdgcn_ds_read_tr16_b64_v4bf16((LAS bf16x4*)(size_t)a); }
DI bf16x8 cat(bf16x4 a, bf16x4 b) { return __builtin_shufflevector(a, b, 0, 1, 2, 3, 4, 5, 6, 7); }
DI bf16x8 rd128(u32 a) { return *(LAS bf16x8*)(size_t)a; }
#define MFMA(a, b, c) __builtin_amdgcn_mfma_f32_32x32x16_bf16((a), (b), (c), 0, 0, 0)

DI u32 gelu2(float a, float b) {
    f32x2 v = {a, b};
    f32x2 v2 = v * v;
    f32x2 t = v2 * (f32x2){-0.10294324f, -0.10294324f} + (f32x2){-2.30220820f, -2.30220820f};
    f32x2 z = t * v;
    f32x2 e = {__builtin_amdgcn_exp2f(z.x), __builtin_amdgcn_exp2f(z.y)};
    f32x2 d = e + (f32x2){1.f, 1.f};
    f32x2 r = {__builtin_amdgcn_rcpf(d.x), __builtin_amdgcn_rcpf(d.y)};
    f32x2 g = v * r;
    return __builtin_bit_cast(u32, __builtin_convertvector(g, bf16x2));
}
DI u32 pack2(float a, float b) { f32x2 g = {a, b}; return __builtin_bit_cast(u32, __builtin_convertvector(g, bf16x2)); }
DI float blo(u32 x) { return __uint_as_float(x << 16); }
DI float bhi(u32 x) { return __uint_as_float(x & 0xffff0000u); }

__global__ __launch_bounds__(256, 1) void mlp_fused(const u16* __restrict__ x, const u16* __restrict__ w1, const u16* __restrict__ b1,
                                                    const u16* __restrict__ w2, const u16* __restrict__ b2, u16* __restrict__ out,
                                                    char* __restrict__ ws) {
    u64 stp[12];
    STAMP(0);
    for (int i_ = 1; i_ < 12; ++i_) stp[i_] = stp[0];
    const u32 tid = threadIdx.x, lane = tid & 63;
    const u32 w = rfl(tid >> 6);
    u32 pfd;
    {
        u64 pc; asm volatile("s_getpc_b64 %0" : "=s"(pc));
        pc &= ~255ull;
        u32 vo = min(tid * 128u, 20480u);
        asm volatile("global_load_dword %0, %1, %2" : "=v"(pfd) : "v"(vo), "s"(pc) : "memory");
    }
    const u32 b = blockIdx.x, xcd = b & 7, j = b >> 3;
    const u32 h = lane >> 5, blk = (lane >> 4) & 1, q_ = (lane & 15) >> 2, p_ = lane & 3, m_ = lane & 31;
    const u32 p1 = (xcd >> 1) * 4 + (j >> 3), tt = j & 7;
    const u32 t1 = (xcd & 1) ? 7 + tt : (tt == 7 ? 15 : tt);
    const u32 rot1 = (tt + 2 * (j >> 3)) & 7;
    const u32 r2 = xcd * 4 + (j >> 3), c2 = j & 7;
    const u32 mi = w >> 1, kj = w & 1;
    const u32 rho = (c2 * 5 + (r2 & 3) * 11) % 43;

    const rsrc_t rX = __builtin_amdgcn_make_buffer_rsrc((void*)x, 0, S_ * D_ * 2, 0x00020000);
    const rsrc_t rW1 = __builtin_amdgcn_make_buffer_rsrc((void*)w1, 0, D_ * F_ * 2, 0x00020000);
    const rsrc_t rW2 = __builtin_amdgcn_make_buffer_rsrc((void*)w2, 0, D_ * F_ * 2, 0x00020000);
    const rsrc_t rB1 = __builtin_amdgcn_make_buffer_rsrc((void*)b1, 0, F_ * 2, 0x00020000);
    const rsrc_t rB2 = __builtin_amdgcn_make_buffer_rsrc((void*)b2, 0, D_ * 2, 0x00020000);
    const rsrc_t rWS = __builtin_amdgcn_make_buffer_rsrc((void*)ws, 0, WS_H + H_BYTES, 0x00020000);
    const rsrc_t rO = __builtin_amdgcn_make_buffer_rsrc((void*)out, 0, S_ * D_ * 2, 0x00020000);

    u32x2 ev;
    { u32 z = 0; asm volatile("buffer_load_dwordx2 %0, %1, %2, 0 offen sc1" : "=v"(ev) : "v"(z), "s"(rWS) : "memory"); }

    const u32 w2v = (lane >> 3) * 1024 + (((lane & 7) ^ (((lane >> 4) & 1) << 2)) * 16);
    auto w2_soff = [&](u32 kjj, u32 i, u32 hs) -> u32 {
        u32 s = i + rho; if (s >= 43) s -= 43; s += 43 * kjj;
        return (16 * s + 8 * hs) * 1024 + c2 * 128;
    };
    auto w2_dma = [&](u32 d) {
        u32 q = d >> 1, kjj = q >= 40 ? 1 : 0, i = 3 + q - 40 * kjj;
        dma16(rW2, w2v, w2_soff(kjj, i, d & 1), d * 1024);
    };
    auto w2_pre = [&](u32 e) {
        u32 kjj = e >= 6 ? 1 : 0, ee = e - 6 * kjj;
        dma16(rW2, w2v, w2_soff(kjj, ee >> 1, e & 1), XTRA + e * 1024);
    };
    const u32 sw2 = (q_ >> 1) & 1;
    const u32 a2base = (4 * h + q_) * 128 + (2 * blk + (p_ >> 1)) * 16 + 8 * (p_ & 1);
    const u32 a2n0 = a2base + 64 * sw2, a2n1 = a2base + 64 * (sw2 ^ 1);
    bf16x8 pre[3][2];
    auto read_pre = [&]() {
#pragma unroll
        for (int i = 0; i < 3; ++i) {
            u32 L = XTRA + (kj * 3 + i) * 2048;
            pre[i][0] = cat(trd(a2n0 + L), trd(a2n0 + L + 1024));
            pre[i][1] = cat(trd(a2n1 + L), trd(a2n1 + L + 1024));
        }
    };
    u64 tag;
    auto make_tag = [&]() {
        u64 e = ((u64)rfl(ev.y) << 32) | rfl(ev.x);
        tag = e * 6364136223846793005ull + 1442695040888963407ull;
    };
    auto store_ack = [&]() {
        if (w == 0) {
            u32x2 tv = {(u32)tag, (u32)(tag >> 32)};
            u32 vo = WS_ACK + b * 8;
            if (lane == 0) asm volatile("buffer_store_dwordx2 %0, %1, %2, 0 offen sc1" ::"v"(tv), "v"(vo), "s"(rWS) : "memory");
        }
    };

    if (t1 != 15) {
        asm volatile("" :: "s"(x), "s"(w1), "s"(b1));
        STAMP(10);
        u32x2 bb[3][4];
#pragma unroll
        for (int u = 0; u < 3; ++u)
#pragma unroll
            for (int g = 0; g < 4; ++g) {
                u32 vo = (96 * t1 + 32 * u + 8 * g + 4 * h) * 2;
                asm volatile("buffer_load_dwordx2 %0, %1, %2, 0 offen" : "=v"(bb[u][g]) : "v"(vo), "s"(rB1) : "memory");
            }
        const u32 xr = lane >> 3, xc = (lane & 7) ^ (xr >> 1);
        const u32 xv0 = xr * 1024 + xc * 16, xv1 = xr * 1024 + (xc ^ 4) * 16;
        u32 wv[3];
#pragma unroll
        for (int i = 0; i < 3; ++i) { u32 p = i * 64 + lane; u32 row = w * 16 + p / 12, c = p % 12; wv[i] = row * 2752 + t1 * 192 + c * 16; }
        auto issue_chunk = [&](int ci) {
            u32 ch = (ci + rot1) & 7;
            u32 slot = (ci % 5) * SLOT;
            u32 xs = p1 * 131072 + w * 32768 + ch * 128;
            dma16(rX, xv0, xs, slot + w * 4096);
            dma16(rX, xv1, xs + 8192, slot + w * 4096 + 1024);
            dma16(rX, xv0, xs + 16384, slot + w * 4096 + 2048);
            dma16(rX, xv1, xs + 24576, slot + w * 4096 + 3072);
            u32 wsf = ch * 64 * 2752;
            dma16(rW1, wv[0], wsf, slot + 16384 + w * 3072);
            dma16(rW1, wv[1], wsf, slot + 16384 + w * 3072 + 1024);
            dma16(rW1, wv[2], wsf, slot + 16384 + w * 3072 + 2048);
        };
#pragma unroll
        for (int ci = 0; ci < 5; ++ci) issue_chunk(ci);
        STAMP(1);
        const u32 xa = (32 * w + m_) * 128 + ((h ^ ((m_ >> 1) & 7)) * 16);
        const u32 wa = 16384 + (8 * h + q_) * 192 + 32 * blk + 8 * p_;
        f32x16 acc[3];
        asm volatile("s_waitcnt vmcnt(35)" : "+v"(ev), "+v"(bb[0][0]), "+v"(bb[0][1]), "+v"(bb[0][2]), "+v"(bb[0][3]), "+v"(bb[1][0]), "+v"(bb[1][1]),
                     "+v"(bb[1][2]), "+v"(bb[1][3]), "+v"(bb[2][0]), "+v"(bb[2][1]), "+v"(bb[2][2]), "+v"(bb[2][3])::"memory");
#pragma unroll
        for (int u = 0; u < 3; ++u)
#pragma unroll
            for (int g = 0; g < 4; ++g) {
                acc[u][4 * g + 0] = blo(bb[u][g].x); acc[u][4 * g + 1] = bhi(bb[u][g].x);
                acc[u][4 * g + 2] = blo(bb[u][g].y); acc[u][4 * g + 3] = bhi(bb[u][g].y);
            }
        asm volatile("" :: "v"(pfd));
        make_tag();
        store_ack();
        auto compute_chunk = [&](int ci) {
            u32 slot = (ci % 5) * SLOT;
#pragma unroll
            for (int s = 0; s < 4; ++s) {
                bf16x8 B = rd128((xa ^ (s << 5)) + slot);
#pragma unroll
                for (int u = 0; u < 3; ++u) {
                    u32 a = wa + slot + s * 3072 + u * 64;
                    bf16x8 A = cat(trd(a), trd(a + 768));
                    acc[u] = MFMA(A, B, acc[u]);
                }
            }
        };
        auto w2_job = [&](int d0, int n) {
            for (int i = 0; i < n; ++i) w2_dma(d0 + w * n + i);
        };
        wait_bar<28>(); STAMP(2);
        for (int e = 0; e < 3; ++e) w2_pre(w * 3 + e);
        compute_chunk(0);
        wait_bar<21>(); issue_chunk(5); compute_chunk(1);
        wait_bar<21>(); issue_chunk(6); compute_chunk(2);
        wait_bar<21>(); issue_chunk(7); compute_chunk(3);
        wait_bar<21>(); w2_job(84, 7); compute_chunk(4);
        wait_bar<21>(); w2_job(112, 7); compute_chunk(5);
        read_pre();
        wait_bar<21>(); w2_job(0, 7); compute_chunk(6);
        wait_bar<21>(); w2_job(28, 7); STAMP(3); compute_chunk(7);
        wait_bar<63>(); w2_job(56, 7); w2_job(140, 5);
        STAMP(4);
        const u32 mt = p1 * 4 + w;
        const u32 nun = (t1 == 14) ? 1 : 3;
#pragma unroll
        for (int u = 0; u < 3; ++u) {
            if (u < (int)nun) {
#pragma unroll
                for (int s = 0; s < 2; ++s) {
                    u32x4 o;
                    o.x = gelu2(acc[u][8 * s + 0], acc[u][8 * s + 1]);
                    o.y = gelu2(acc[u][8 * s + 2], acc[u][8 * s + 3]);
                    o.z = gelu2(acc[u][8 * s + 4], acc[u][8 * s + 5]);
                    o.w = gelu2(acc[u][8 * s + 6], acc[u][8 * s + 7]);
                    u32 so = WS_H + (mt * 86 + 2 * (3 * t1 + u) + s) * 1024;
                    u32 vo = lane * 16;
                    asm volatile("buffer_store_dwordx4 %0, %1, %2, %3 offen sc1\n\ts_nop 1" ::"v"(o), "v"(vo), "s"(rWS), "s"(so) : "memory");
                }
            }
        }
        STAMP(5);
        asm volatile("s_waitcnt vmcnt(0)" ::: "memory");
        {
            u32x2 tv = {(u32)tag, (u32)(tag >> 32)};
            u32 vo = WS_FLAG + (mt * 16 + t1) * 128;
            if (lane == 0) asm volatile("buffer_store_dwordx2 %0, %1, %2, 0 offen sc1" ::"v"(tv), "v"(vo), "s"(rWS) : "memory");
        }
        STAMP(6);
    } else {
        for (int e = 0; e < 3; ++e) w2_pre(w * 3 + e);
        asm volatile("s_waitcnt vmcnt(0)" : "+v"(ev)::"memory");
        asm volatile("" :: "v"(pfd));
        make_tag();
        store_ack();
        wait_bar<63>();
        read_pre();
        wait_bar<63>();
        for (int i = 0; i < 40; ++i) w2_dma(w * 40 + i);
        if (p1 == 0 && w == 3) {
            bool ok = false;
            for (u32 spin = 0; spin < SPIN_CAP && !ok; ++spin) {
                u32x4 a0, a1; u32 vo = WS_ACK + lane * 32;
                asm volatile("buffer_load_dwordx4 %0, %2, %3, 0 offen sc1\n\tbuffer_load_dwordx4 %1, %2, %3, 0 offen offset:16 sc1\n\ts_waitcnt vmcnt(0)"
                             : "=&v"(a0), "=&v"(a1) : "v"(vo), "s"(rWS) : "memory");
                u32 lo = (u32)tag, hi = (u32)(tag >> 32);
                bool m = a0.x == lo && a0.y == hi && a0.z == lo && a0.w == hi && a1.x == lo && a1.y == hi && a1.z == lo && a1.w == hi;
                ok = __all(m);
                if (!ok) __builtin_amdgcn_s_sleep(8);
            }
            if (ok) {
                u32x2 tv = {(u32)tag, (u32)(tag >> 32)}; u32 vo = 0;
                if (lane == 0) asm volatile("buffer_store_dwordx2 %0, %1, %2, 0 offen sc1" ::"v"(tv), "v"(vo), "s"(rWS) : "memory");
            }
        }
        asm volatile("s_waitcnt vmcnt(0)" ::: "memory");
    }
    u32x2 b2v[4];
#pragma unroll
    for (int g = 0; g < 4; ++g) {
        u32 vo = (64 * c2 + 32 * kj + 8 * g + 4 * h) * 2;
        asm volatile("buffer_load_dwordx2 %0, %1, %2, 0 offen" : "=v"(b2v[g]) : "v"(vo), "s"(rB2) : "memory");
    }
    wait_bar<63>();
    const u32 mt2 = r2 * 2 + mi;
    {
        u32 vo = WS_FLAG + (mt2 * 16 + (kj ? 7 : 0) + (lane & 7)) * 128;
        u32 lo = (u32)tag, hi = (u32)(tag >> 32);
        for (u32 spin = 0; spin < SPIN_CAP; ++spin) {
            u32x2 f;
            asm volatile("buffer_load_dwordx2 %0, %1, %2, 0 offen sc1\n\ts_waitcnt vmcnt(0)" : "=&v"(f) : "v"(vo), "s"(rWS) : "memory");
            if (__all(f.x == lo && f.y == hi)) break;
            __builtin_amdgcn_s_sleep(1);
        }
    }
    STAMP(7);
    u32x4 hf[43];
    {
        u32 vo = lane * 16;
        u32 base = WS_H + (mt2 * 86 + 43 * kj) * 1024;
        u32 s = rho;
#pragma unroll
        for (int i = 0; i < 43; ++i) {
            u32 so = base + s * 1024;
            asm volatile("buffer_load_dwordx4 %0, %1, %2, %3 offen sc1" : "=v"(hf[i]) : "v"(vo), "s"(rWS), "s"(so) : "memory");
            s = (s == 42) ? 0 : s + 1;
        }
    }
    f32x16 o0, o1;
    asm volatile("" : "+v"(b2v[0]), "+v"(b2v[1]), "+v"(b2v[2]), "+v"(b2v[3]));
    {
        f32x16 bi;
#pragma unroll
        for (int g = 0; g < 4; ++g) { bi[4 * g] = blo(b2v[g].x); bi[4 * g + 1] = bhi(b2v[g].x); bi[4 * g + 2] = blo(b2v[g].y); bi[4 * g + 3] = bhi(b2v[g].y); }
#pragma unroll
        for (int i = 0; i < 16; ++i) { o0[i] = kj ? 0.f : bi[i]; o1[i] = kj ? bi[i] : 0.f; }
    }
#define STEP(i)                                                                                          \
    {                                                                                                    \
        bf16x8 A0, A1;                                                                                   \
        if (i < 3) { A0 = pre[i < 3 ? i : 0][0]; A1 = pre[i < 3 ? i : 0][1]; }                            \
        else {                                                                                           \
            u32 L = (kj * 40 + (i - 3)) * 2048;                                                          \
            A0 = cat(trd(a2n0 + L), trd(a2n0 + L + 1024));                                               \
            A1 = cat(trd(a2n1 + L), trd(a2n1 + L + 1024));                                               \
        }                                                                                                \
        asm volatile("s_waitcnt vmcnt(%1)" : "+v"(hf[i]) : "i"(42 - i));                                 \
        bf16x8 B = __builtin_bit_cast(bf16x8, hf[i]);                                                    \
        o0 = MFMA(A0, B, o0);                                                                            \
        o1 = MFMA(A1, B, o1);                                                                            \
    }
    STEP(0) STEP(1) STEP(2) STEP(3) STEP(4) STEP(5) STEP(6) STEP(7) STEP(8) STEP(9)
    STEP(10) STEP(11) STEP(12) STEP(13) STEP(14) STEP(15) STEP(16) STEP(17) STEP(18) STEP(19)
    STEP(20) STEP(21) STEP(22) STEP(23) STEP(24) STEP(25) STEP(26) STEP(27) STEP(28) STEP(29)
    STEP(30) STEP(31) STEP(32) STEP(33) STEP(34) STEP(35) STEP(36) STEP(37) STEP(38) STEP(39)
    STEP(40) STEP(41) STEP(42)
    STAMP(8);
    wait_bar<63>();
    {
        f32x16 snd = kj ? o0 : o1;
        u32 a = w * 4096 + lane * 16;
#pragma unroll
        for (int g = 0; g < 4; ++g) {
            typedef __attribute__((ext_vector_type(4))) float f32x4;
            f32x4 v = {snd[4 * g], snd[4 * g + 1], snd[4 * g + 2], snd[4 * g + 3]};
            *(LAS f32x4*)(size_t)(a + g * 1024) = v;
        }
    }
    wait_bar<63>();
    {
        f32x16 kp = kj ? o1 : o0;
        u32 a = (w ^ 1) * 4096 + lane * 16;
        u32 d[8];
#pragma unroll
        for (int g = 0; g < 4; ++g) {
            typedef __attribute__((ext_vector_type(4))) float f32x4;
            f32x4 v = *(LAS f32x4*)(size_t)(a + g * 1024);
            d[2 * g] = pack2(kp[4 * g] + v[0], kp[4 * g + 1] + v[1]);
            d[2 * g + 1] = pack2(kp[4 * g + 2] + v[2], kp[4 * g + 3] + v[3]);
        }
        u32 row = r2 * 64 + mi * 32 + m_;
        u32 vo = row * 1024 + (c2 * 64 + kj * 32) * 2 + h * 16;
#pragma unroll
        for (int k = 0; k < 2; ++k) {
            auto r0 = __builtin_amdgcn_permlane32_swap(d[4 * k], d[4 * k + 2], false, false);
            auto r1 = __builtin_amdgcn_permlane32_swap(d[4 * k + 1], d[4 * k + 3], false, false);
            u32x4 o = {r0[0], r1[0], r0[1], r1[1]};
            u32 so = k * 32;
            asm volatile("buffer_store_dwordx4 %0, %1, %2, %3 offen\n\ts_nop 1" ::"v"(o), "v"(vo), "s"(rO), "s"(so) : "memory");
        }
    }
    STAMP(9);
    if (threadIdx.x == 0) {
        u64 dt = stp[PROBE_TO] - stp[PROBE_FROM];
        if (dt > 10000) dt = 10000;
        u64 until = stp[9] + 5 * dt, now;
        do { asm volatile("s_memrealtime %0\n\ts_waitcnt lgkmcnt(0)" : "=s"(now) :: "memory"); } while (now < until);
    }
}

extern "C" void kernel_launch(void* const* d_in, const int* in_sizes, int n_in, void* d_out, int out_size, void* d_ws, size_t ws_size,
                              hipStream_t stream) {
    (void)in_sizes; (void)n_in; (void)out_size; (void)ws_size;
    hipFuncSetAttribute((const void*)mlp_fused, hipFuncAttributeMaxDynamicSharedMemorySize, 163840);
    mlp_fused<<<256, 256, 163840, stream>>>((const u16*)d_in[0], (const u16*)d_in[1], (const u16*)d_in[2], (const u16*)d_in[3],
                                            (const u16*)d_in[4], (u16*)d_out, (char*)d_ws);
}
```

```cpp
#define PROBE_FROM 0
#define PROBE_TO 9
#define CODEPF 1
#include <hip/hip_runtime.h>
#include <hip/hip_bf16.h>
#include <stdint.h>

typedef unsigned short u16;
typedef unsigned u32;
typedef unsigned long long u64;
typedef __attribute__((ext_vector_type(4))) unsigned u32x4;
typedef __attribute__((ext_vector_type(2))) unsigned u32x2;
typedef __attribute__((ext_vector_type(4))) __bf16 bf16x4;
typedef __attribute__((ext_vector_type(8))) __bf16 bf16x8;
typedef __attribute__((ext_vector_type(2))) __bf16 bf16x2;
typedef __attribute__((ext_vector_type(2))) float f32x2;
typedef __attribute__((ext_vector_type(16))) float f32x16;
typedef __amdgpu_buffer_rsrc_t rsrc_t;
#define LAS __attribute__((address_space(3)))
#define DI __device__ __forceinline__

constexpr int S_ = 2048, D_ = 512, F_ = 1376;
constexpr u32 SLOT = 28672, XTRA = 5 * SLOT;
constexpr u32 WS_ACK = 4096, WS_FLAG = 8192, WS_H = 1u << 20;
constexpr u32 H_BYTES = 64u * 86u * 1024u;
constexpr u32 SPIN_CAP = 1u << 17;

#define STAMP(i) do { if (threadIdx.x == 0) { u64 t_; asm volatile("s_memrealtime %0\n\ts_waitcnt lgkmcnt(0)" : "=s"(t_) :: "memory"); stp[i] = t_; } } while (0)

DI u32 rfl(u32 x) { return __builtin_amdgcn_readfirstlane(x); }
DI void dma16(rsrc_t rs, u32 voff, u32 soff, u32 lds) {
    asm volatile("s_mov_b32 m0, %0\n\ts_nop 0\n\tbuffer_load_dwordx4 %1, %2, %3 offen lds" ::"s"(lds), "v"(voff), "s"(rs), "s"(soff) : "memory");
}
template <int N> DI void wait_bar() { asm volatile("s_waitcnt vmcnt(%0) lgkmcnt(0)\n\ts_barrier" ::"i"(N) : "memory"); }
DI bf16x4 trd(u32 a) { return __builtin_amdgcn_ds_read_tr16_b64_v4bf16((LAS bf16x4*)(size_t)a); }
DI bf16x8 cat(bf16x4 a, bf16x4 b) { return __builtin_shufflevector(a, b, 0, 1, 2, 3, 4, 5, 6, 7); }
DI bf16x8 rd128(u32 a) { return *(LAS bf16x8*)(size_t)a; }
#define MFMA(a, b, c) __builtin_amdgcn_mfma_f32_32x32x16_bf16((a), (b), (c), 0, 0, 0)

DI u32 gelu2(float a, float b) {
    f32x2 v = {a, b};
    f32x2 v2 = v * v;
    f32x2 t = v2 * (f32x2){-0.10294324f, -0.10294324f} + (f32x2){-2.30220820f, -2.30220820f};
    f32x2 z = t * v;
    f32x2 e = {__builtin_amdgcn_exp2f(z.x), __builtin_amdgcn_exp2f(z.y)};
    f32x2 d = e + (f32x2){1.f, 1.f};
    f32x2 r = {__builtin_amdgcn_rcpf(d.x), __builtin_amdgcn_rcpf(d.y)};
    f32x2 g = v * r;
    return __builtin_bit_cast(u32, __builtin_convertvector(g, bf16x2));
}
DI u32 pack2(float a, float b) { f32x2 g = {a, b}; return __builtin_bit_cast(u32, __builtin_convertvector(g, bf16x2)); }
DI float blo(u32 x) { return __uint_as_float(x << 16); }
DI float bhi(u32 x) { return __uint_as_float(x & 0xffff0000u); }

__global__ __launch_bounds__(256, 1) void mlp_fused(const u16* __restrict__ x, const u16* __restrict__ w1, const u16* __restrict__ b1,
                                                    const u16* __restrict__ w2, const u16* __restrict__ b2, u16* __restrict__ out,
                                                    char* __restrict__ ws) {
    u64 stp[12];
    STAMP(0);
    for (int i_ = 1; i_ < 12; ++i_) stp[i_] = stp[0];
    const u32 tid = threadIdx.x, lane = tid & 63;
    const u32 w = rfl(tid >> 6);
    u32 pfd;
    {
        u64 pc; asm volatile("s_getpc_b64 %0" : "=s"(pc));
        pc &= ~255ull;
        u32 vo = min(tid * 128u, 20480u);
        asm volatile("global_load_dword %0, %1, %2" : "=v"(pfd) : "v"(vo), "s"(pc) : "memory");
    }
    const u32 b = blockIdx.x, xcd = b & 7, j = b >> 3;
    const u32 h = lane >> 5, blk = (lane >> 4) & 1, q_ = (lane & 15) >> 2, p_ = lane & 3, m_ = lane & 31;
    const u32 p1 = (xcd >> 1) * 4 + (j >> 3), tt = j & 7;
    const u32 t1 = (xcd & 1) ? 7 + tt : (tt == 7 ? 15 : tt);
    const u32 rot1 = (tt + 2 * (j >> 3)) & 7;
    const u32 r2 = xcd * 4 + (j >> 3), c2 = j & 7;
    const u32 mi = w >> 1, kj = w & 1;
    const u32 rho = (c2 * 5 + (r2 & 3) * 11) % 43;

    const rsrc_t rX = __builtin_amdgcn_make_buffer_rsrc((void*)x, 0, S_ * D_ * 2, 0x00020000);
    const rsrc_t rW1 = __builtin_amdgcn_make_buffer_rsrc((void*)w1, 0, D_ * F_ * 2, 0x00020000);
    const rsrc_t rW2 = __builtin_amdgcn_make_buffer_rsrc((void*)w2, 0, D_ * F_ * 2, 0x00020000);
    const rsrc_t rB1 = __builtin_amdgcn_make_buffer_rsrc((void*)b1, 0, F_ * 2, 0x00020000);
    const rsrc_t rB2 = __builtin_amdgcn_make_buffer_rsrc((void*)b2, 0, D_ * 2, 0x00020000);
    const rsrc_t rWS = __builtin_amdgcn_make_buffer_rsrc((void*)ws, 0, WS_H + H_BYTES, 0x00020000);
    const rsrc_t rO = __builtin_amdgcn_make_buffer_rsrc((void*)out, 0, S_ * D_ * 2, 0x00020000);

    u32x2 ev;
    { u32 z = 0; asm volatile("buffer_load_dwordx2 %0, %1, %2, 0 offen sc1" : "=v"(ev) : "v"(z), "s"(rWS) : "memory"); }

    const u32 w2v = (lane >> 3) * 1024 + (((lane & 7) ^ (((lane >> 4) & 1) << 2)) * 16);
    auto w2_soff = [&](u32 kjj, u32 i, u32 hs) -> u32 {
        u32 s = i + rho; if (s >= 43) s -= 43; s += 43 * kjj;
        return (16 * s + 8 * hs) * 1024 + c2 * 128;
    };
    auto w2_dma = [&](u32 d) {
        u32 q = d >> 1, kjj = q >= 40 ? 1 : 0, i = 3 + q - 40 * kjj;
        dma16(rW2, w2v, w2_soff(kjj, i, d & 1), d * 1024);
    };
    auto w2_pre = [&](u32 e) {
        u32 kjj = e >= 6 ? 1 : 0, ee = e - 6 * kjj;
        dma16(rW2, w2v, w2_soff(kjj, ee >> 1, e & 1), XTRA + e * 1024);
    };
    const u32 sw2 = (q_ >> 1) & 1;
    const u32 a2base = (4 * h + q_) * 128 + (2 * blk + (p_ >> 1)) * 16 + 8 * (p_ & 1);
    const u32 a2n0 = a2base + 64 * sw2, a2n1 = a2base + 64 * (sw2 ^ 1);
    bf16x8 pre[3][2];
    auto read_pre = [&]() {
#pragma unroll
        for (int i = 0; i < 3; ++i) {
            u32 L = XTRA + (kj * 3 + i) * 2048;
            pre[i][0] = cat(trd(a2n0 + L), trd(a2n0 + L + 1024));
            pre[i][1] = cat(trd(a2n1 + L), trd(a2n1 + L + 1024));
        }
    };
    u64 tag;
    auto make_tag = [&]() {
        u64 e = ((u64)rfl(ev.y) << 32) | rfl(ev.x);
        tag = e * 6364136223846793005ull + 1442695040888963407ull;
    };
    auto store_ack = [&]() {
        if (w == 0) {
            u32x2 tv = {(u32)tag, (u32)(tag >> 32)};
            u32 vo = WS_ACK + b * 8;
            if (lane == 0) asm volatile("buffer_store_dwordx2 %0, %1, %2, 0 offen sc1" ::"v"(tv), "v"(vo), "s"(rWS) : "memory");
        }
    };

    if (t1 != 15) {
        asm volatile("" :: "s"(x), "s"(w1), "s"(b1));
        STAMP(10);
        u32x2 bb[3][4];
#pragma unroll
        for (int u = 0; u < 3; ++u)
#pragma unroll
            for (int g = 0; g < 4; ++g) {
                u32 vo = (96 * t1 + 32 * u + 8 * g + 4 * h) * 2;
                asm volatile("buffer_load_dwordx2 %0, %1, %2, 0 offen" : "=v"(bb[u][g]) : "v"(vo), "s"(rB1) : "memory");
            }
        const u32 xr = lane >> 3, xc = (lane & 7) ^ (xr >> 1);
        const u32 xv0 = xr * 1024 + xc * 16, xv1 = xr * 1024 + (xc ^ 4) * 16;
        u32 wv[3];
#pragma unroll
        for (int i = 0; i < 3; ++i) { u32 p = i * 64 + lane; u32 row = w * 16 + p / 12, c = p % 12; wv[i] = row * 2752 + t1 * 192 + c * 16; }
        auto issue_chunk = [&](int ci) {
            u32 ch = (ci + rot1) & 7;
            u32 slot = (ci % 5) * SLOT;
            u32 xs = p1 * 131072 + w * 32768 + ch * 128;
            dma16(rX, xv0, xs, slot + w * 4096);
            dma16(rX, xv1, xs + 8192, slot + w * 4096 + 1024);
            dma16(rX, xv0, xs + 16384, slot + w * 4096 + 2048);
            dma16(rX, xv1, xs + 24576, slot + w * 4096 + 3072);
            u32 wsf = ch * 64 * 2752;
            dma16(rW1, wv[0], wsf, slot + 16384 + w * 3072);
            dma16(rW1, wv[1], wsf, slot + 16384 + w * 3072 + 1024);
            dma16(rW1, wv[2], wsf, slot + 16384 + w * 3072 + 2048);
        };
#pragma unroll
        for (int ci = 0; ci < 5; ++ci) issue_chunk(ci);
        STAMP(1);
        const u32 xa = (32 * w + m_) * 128 + ((h ^ ((m_ >> 1) & 7)) * 16);
        const u32 wa = 16384 + (8 * h + q_) * 192 + 32 * blk + 8 * p_;
        f32x16 acc[3];
        asm volatile("s_waitcnt vmcnt(35)" : "+v"(ev), "+v"(bb[0][0]), "+v"(bb[0][1]), "+v"(bb[0][2]), "+v"(bb[0][3]), "+v"(bb[1][0]), "+v"(bb[1][1]),
                     "+v"(bb[1][2]), "+v"(bb[1][3]), "+v"(bb[2][0]), "+v"(bb[2][1]), "+v"(bb[2][2]), "+v"(bb[2][3])::"memory");
#pragma unroll
        for (int u = 0; u < 3; ++u)
#pragma unroll
            for (int g = 0; g < 4; ++g) {
                acc[u][4 * g + 0] = blo(bb[u][g].x); acc[u][4 * g + 1] = bhi(bb[u][g].x);
                acc[u][4 * g + 2] = blo(bb[u][g].y); acc[u][4 * g + 3] = bhi(bb[u][g].y);
            }
        asm volatile("" :: "v"(pfd));
        make_tag();
        store_ack();
        auto compute_chunk = [&](int ci) {
            u32 slot = (ci % 5) * SLOT;
#pragma unroll
            for (int s = 0; s < 4; ++s) {
                bf16x8 B = rd128((xa ^ (s << 5)) + slot);
#pragma unroll
                for (int u = 0; u < 3; ++u) {
                    u32 a = wa + slot + s * 3072 + u * 64;
                    bf16x8 A = cat(trd(a), trd(a + 768));
                    acc[u] = MFMA(A, B, acc[u]);
                }
            }
        };
        auto w2_job = [&](int d0, int n) {
            for (int i = 0; i < n; ++i) w2_dma(d0 + w * n + i);
        };
        wait_bar<28>(); STAMP(2);
        for (int e = 0; e < 3; ++e) w2_pre(w * 3 + e);
        compute_chunk(0);
        wait_bar<21>(); issue_chunk(5); compute_chunk(1);
        wait_bar<21>(); issue_chunk(6); compute_chunk(2);
        wait_bar<21>(); issue_chunk(7); compute_chunk(3);
        wait_bar<21>(); w2_job(84, 7); compute_chunk(4);
        wait_bar<21>(); w2_job(112, 7); compute_chunk(5);
        read_pre();
        wait_bar<21>(); w2_job(0, 7); compute_chunk(6);
        wait_bar<21>(); w2_job(28, 7); STAMP(3); compute_chunk(7);
        wait_bar<63>(); w2_job(56, 7); w2_job(140, 5);
        STAMP(4);
        const u32 mt = p1 * 4 + w;
        const u32 nun = (t1 == 14) ? 1 : 3;
#pragma unroll
        for (int u = 0; u < 3; ++u) {
            if (u < (int)nun) {
#pragma unroll
                for (int s = 0; s < 2; ++s) {
                    u32x4 o;
                    o.x = gelu2(acc[u][8 * s + 0], acc[u][8 * s + 1]);
                    o.y = gelu2(acc[u][8 * s + 2], acc[u][8 * s + 3]);
                    o.z = gelu2(acc[u][8 * s + 4], acc[u][8 * s + 5]);
                    o.w = gelu2(acc[u][8 * s + 6], acc[u][8 * s + 7]);
                    u32 so = WS_H + (mt * 86 + 2 * (3 * t1 + u) + s) * 1024;
                    u32 vo = lane * 16;
                    asm volatile("buffer_store_dwordx4 %0, %1, %2, %3 offen sc1\n\ts_nop 1" ::"v"(o), "v"(vo), "s"(rWS), "s"(so) : "memory");
                }
            }
        }
        STAMP(5);
        asm volatile("s_waitcnt vmcnt(0)" ::: "memory");
        {
            u32x2 tv = {(u32)tag, (u32)(tag >> 32)};
            u32 vo = WS_FLAG + (mt * 16 + t1) * 128;
            if (lane == 0) asm volatile("buffer_store_dwordx2 %0, %1, %2, 0 offen sc1" ::"v"(tv), "v"(vo), "s"(rWS) : "memory");
        }
        STAMP(6);
    } else {
        for (int e = 0; e < 3; ++e) w2_pre(w * 3 + e);
        asm volatile("s_waitcnt vmcnt(0)" : "+v"(ev)::"memory");
        asm volatile("" :: "v"(pfd));
        make_tag();
        store_ack();
        wait_bar<63>();
        read_pre();
        wait_bar<63>();
        for (int i = 0; i < 40; ++i) w2_dma(w * 40 + i);
        if (p1 == 0 && w == 3) {
            bool ok = false;
            for (u32 spin = 0; spin < SPIN_CAP && !ok; ++spin) {
                u32x4 a0, a1; u32 vo = WS_ACK + lane * 32;
                asm volatile("buffer_load_dwordx4 %0, %2, %3, 0 offen sc1\n\tbuffer_load_dwordx4 %1, %2, %3, 0 offen offset:16 sc1\n\ts_waitcnt vmcnt(0)"
                             : "=&v"(a0), "=&v"(a1) : "v"(vo), "s"(rWS) : "memory");
                u32 lo = (u32)tag, hi = (u32)(tag >> 32);
                bool m = a0.x == lo && a0.y == hi && a0.z == lo && a0.w == hi && a1.x == lo && a1.y == hi && a1.z == lo && a1.w == hi;
                ok = __all(m);
                if (!ok) __builtin_amdgcn_s_sleep(8);
            }
            if (ok) {
                u32x2 tv = {(u32)tag, (u32)(tag >> 32)}; u32 vo = 0;
                if (lane == 0) asm volatile("buffer_store_dwordx2 %0, %1, %2, 0 offen sc1" ::"v"(tv), "v"(vo), "s"(rWS) : "memory");
            }
        }
        asm volatile("s_waitcnt vmcnt(0)" ::: "memory");
    }
    u32x2 b2v[4];
#pragma unroll
    for (int g = 0; g < 4; ++g) {
        u32 vo = (64 * c2 + 32 * kj + 8 * g + 4 * h) * 2;
        asm volatile("buffer_load_dwordx2 %0, %1, %2, 0 offen" : "=v"(b2v[g]) : "v"(vo), "s"(rB2) : "memory");
    }
    wait_bar<63>();
    const u32 mt2 = r2 * 2 + mi;
    {
        u32 vo = WS_FLAG + (mt2 * 16 + (kj ? 7 : 0) + (lane & 7)) * 128;
        u32 lo = (u32)tag, hi = (u32)(tag >> 32);
        for (u32 spin = 0; spin < SPIN_CAP; ++spin) {
            u32x2 f;
            asm volatile("buffer_load_dwordx2 %0, %1, %2, 0 offen sc1\n\ts_waitcnt vmcnt(0)" : "=&v"(f) : "v"(vo), "s"(rWS) : "memory");
            if (__all(f.x == lo && f.y == hi)) break;
            __builtin_amdgcn_s_sleep(1);
        }
    }
    STAMP(7);
    u32x4 hf[43];
    {
        u32 vo = lane * 16;
        u32 base = WS_H + (mt2 * 86 + 43 * kj) * 1024;
        u32 s = rho;
#pragma unroll
        for (int i = 0; i < 43; ++i) {
            u32 so = base + s * 1024;
            asm volatile("buffer_load_dwordx4 %0, %1, %2, %3 offen sc1" : "=v"(hf[i]) : "v"(vo), "s"(rWS), "s"(so) : "memory");
            s = (s == 42) ? 0 : s + 1;
        }
    }
    f32x16 o0, o1;
    asm volatile("" : "+v"(b2v[0]), "+v"(b2v[1]), "+v"(b2v[2]), "+v"(b2v[3]));
    {
        f32x16 bi;
#pragma unroll
        for (int g = 0; g < 4; ++g) { bi[4 * g] = blo(b2v[g].x); bi[4 * g + 1] = bhi(b2v[g].x); bi[4 * g + 2] = blo(b2v[g].y); bi[4 * g + 3] = bhi(b2v[g].y); }
#pragma unroll
        for (int i = 0; i < 16; ++i) { o0[i] = kj ? 0.f : bi[i]; o1[i] = kj ? bi[i] : 0.f; }
    }
#define STEP(i)                                                                                          \
    {                                                                                                    \
        bf16x8 A0, A1;                                                                                   \
        if (i < 3) { A0 = pre[i < 3 ? i : 0][0]; A1 = pre[i < 3 ? i : 0][1]; }                            \
        else {                                                                                           \
            u32 L = (kj * 40 + (i - 3)) * 2048;                                                          \
            A0 = cat(trd(a2n0 + L), trd(a2n0 + L + 1024));                                               \
            A1 = cat(trd(a2n1 + L), trd(a2n1 + L + 1024));                                               \
        }                                                                                                \
        asm volatile("s_waitcnt vmcnt(%1)" : "+v"(hf[i]) : "i"(42 - i));                                 \
        bf16x8 B = __builtin_bit_cast(bf16x8, hf[i]);                                                    \
        o0 = MFMA(A0, B, o0);                                                                            \
        o1 = MFMA(A1, B, o1);                                                                            \
    }
    STEP(0) STEP(1) STEP(2) STEP(3) STEP(4) STEP(5) STEP(6) STEP(7) STEP(8) STEP(9)
    STEP(10) STEP(11) STEP(12) STEP(13) STEP(14) STEP(15) STEP(16) STEP(17) STEP(18) STEP(19)
    STEP(20) STEP(21) STEP(22) STEP(23) STEP(24) STEP(25) STEP(26) STEP(27) STEP(28) STEP(29)
    STEP(30) STEP(31) STEP(32) STEP(33) STEP(34) STEP(35) STEP(36) STEP(37) STEP(38) STEP(39)
    STEP(40) STEP(41) STEP(42)
    STAMP(8);
    wait_bar<63>();
    {
        f32x16 snd = kj ? o0 : o1;
        u32 a = w * 4096 + lane * 16;
#pragma unroll
        for (int g = 0; g < 4; ++g) {
            typedef __attribute__((ext_vector_type(4))) float f32x4;
            f32x4 v = {snd[4 * g], snd[4 * g + 1], snd[4 * g + 2], snd[4 * g + 3]};
            *(LAS f32x4*)(size_t)(a + g * 1024) = v;
        }
    }
    wait_bar<63>();
    {
        f32x16 kp = kj ? o1 : o0;
        u32 a = (w ^ 1) * 4096 + lane * 16;
        u32 d[8];
#pragma unroll
        for (int g = 0; g < 4; ++g) {
            typedef __attribute__((ext_vector_type(4))) float f32x4;
            f32x4 v = *(LAS f32x4*)(size_t)(a + g * 1024);
            d[2 * g] = pack2(kp[4 * g] + v[0], kp[4 * g + 1] + v[1]);
            d[2 * g + 1] = pack2(kp[4 * g + 2] + v[2], kp[4 * g + 3] + v[3]);
        }
        u32 row = r2 * 64 + mi * 32 + m_;
        u32 vo = row * 1024 + (c2 * 64 + kj * 32) * 2 + h * 16;
#pragma unroll
        for (int k = 0; k < 2; ++k) {
            auto r0 = __builtin_amdgcn_permlane32_swap(d[4 * k], d[4 * k + 2], false, false);
            auto r1 = __builtin_amdgcn_permlane32_swap(d[4 * k + 1], d[4 * k + 3], false, false);
            u32x4 o = {r0[0], r1[0], r0[1], r1[1]};
            u32 so = k * 32;
            asm volatile("buffer_store_dwordx4 %0, %1, %2, %3 offen\n\ts_nop 1" ::"v"(o), "v"(vo), "s"(rO), "s"(so) : "memory");
        }
    }
    STAMP(9);
}

__global__ void k_pre(char* ws) { if (threadIdx.x == 0) { u64 t; asm volatile("s_memrealtime %0\n\ts_waitcnt lgkmcnt(0)" : "=s"(t) :: "memory"); *(u64*)(ws + (100u << 20)) = t; } }
__global__ void k_post(char* ws) {
    u64 t0; asm volatile("s_memrealtime %0\n\ts_waitcnt lgkmcnt(0)" : "=s"(t0) :: "memory");
    const u64* e = (const u64*)(ws + (101u << 20));
    u64 m = e[threadIdx.x];
    for (int o = 32; o > 0; o >>= 1) { u64 x = __shfl_xor(m, o); m = x > m ? x : m; }
    __shared__ u64 sm[4]; if ((threadIdx.x & 63) == 0) sm[threadIdx.x >> 6] = m; __syncthreads();
    for (int i = 0; i < 4; ++i) m = sm[i] > m ? sm[i] : m;
    if (m == 12345) ws[0] = 1;
}
extern "C" void kernel_launch(void* const* d_in, const int* in_sizes, int n_in, void* d_out, int out_size, void* d_ws, size_t ws_size,
                              hipStream_t stream) {
    (void)in_sizes; (void)n_in; (void)out_size; (void)ws_size;
    hipFuncSetAttribute((const void*)mlp_fused, hipFuncAttributeMaxDynamicSharedMemorySize, 163840);
    mlp_fused<<<256, 256, 163840, stream>>>((const u16*)d_in[0], (const u16*)d_in[1], (const u16*)d_in[2], (const u16*)d_in[3],
                                            (const u16*)d_in[4], (u16*)d_out, (char*)d_ws);
}
```

```cpp
#include <hip/hip_runtime.h>
#include <stdint.h>

__global__ __launch_bounds__(384, 2) void mlp_fused(const void* x, const void* w1, const void* b1, const void* w2, const void* b2,
                                                    void* out, void* ws) {
    asm volatile(
        "s_load_dwordx8 s[4:11], %0, 0\n"
        "s_load_dwordx4 s[12:15], %0, 32\n"
        "s_load_dwordx2 s[16:17], %0, 48\n"
        "v_lshrrev_b32 v2, 6, %2\n"
        "v_and_b32 v1, 63, %2\n"
        "s_and_b32 s64, %1, 7\n"
        "v_readfirstlane_b32 s48, v2\n"
        "s_lshr_b32 s65, %1, 3\n"
        "s_lshr_b32 s58, s65, 3\n"
        "s_and_b32 s56, s65, 7\n"
        "s_lshr_b32 s50, s64, 1\n"
        "s_lshl_b32 s50, s50, 2\n"
        "s_add_u32 s50, s50, s58\n"
        "s_and_b32 s51, s64, 1\n"
        "s_lshl_b32 s51, s51, 3\n"
        "s_add_u32 s51, s51, s56\n"
        "s_mul_i32 s66, s51, 3\n"
        "s_lshl_b32 s67, s51, 1\n"
        "s_add_u32 s67, s67, 11\n"
        "s_cmp_lt_u32 s51, 11\n"
        "s_cselect_b32 s52, s66, s67\n"
        "s_cselect_b32 s53, 3, 2\n"
        "s_lshl_b32 s55, s64, 2\n"
        "s_add_u32 s55, s55, s58\n"
        "s_cmp_ge_u32 s48, 4\n"
        "s_cbranch_scc1 Lloader_%=\n"
        "s_getpc_b64 s[92:93]\n"
        "v_lshlrev_b32 v3, 7, %2\n"
        "s_and_b32 s92, s92, 0xffffff00\n"
        "v_min_u32 v3, 12288, v3\n"
        "global_load_dword v4, v3, s[92:93]\n"
        "v_lshrrev_b32 v2, 5, v1\n"
        "v_lshlrev_b32 v10, 4, v2\n"
        "s_lshl_b32 s64, s52, 6\n"
        "s_mov_b32 s42, 6692864\n"
        "s_mov_b32 s43, 0x20000\n"
        "s_mov_b32 s34, 2752\n"
        "s_mov_b32 s35, 0x20000\n"
        "s_mul_i32 s66, s56, 5\n"
        "s_mul_i32 s67, s58, 11\n"
        "s_add_u32 s57, s66, s67\n"
        "s_sub_u32 s66, s57, 43\n"
        "s_cselect_b32 s57, s57, s66\n"
        "s_and_b32 s87, s48, 1\n"
        "s_lshr_b32 s88, s48, 1\n"
        "v_bfe_u32 v3, v1, 4, 1\n"
        "v_bfe_u32 v5, v1, 2, 2\n"
        "v_and_b32 v6, 3, v1\n"
        "v_and_b32 v7, 31, v1\n"
        "v_lshlrev_b32 v21, 4, v1\n"
        "v_lshlrev_b32 v8, 5, v3\n"
        "v_and_b32 v9, 1, v6\n"
        "v_lshl_add_u32 v8, v9, 4, v8\n"
        "v_lshrrev_b32 v9, 1, v6\n"
        "v_lshl_add_u32 v8, v9, 3, v8\n"
        "v_lshl_add_u32 v9, v2, 3, v5\n"
        "v_mul_u32_u24 v15, 192, v9\n"
        "v_add_u32 v15, v15, v8\n"
        "v_add_u32 v15, 16384, v15\n"
        "v_lshlrev_b32 v11, 7, v9\n"
        "v_bfe_u32 v12, v5, 1, 1\n"
        "v_xor_b32 v12, s87, v12\n"
        "v_lshlrev_b32 v12, 6, v12\n"
        "v_or_b32 v11, v11, v8\n"
        "v_xor_b32 v18, v11, v12\n"
        "v_xor_b32 v17, 64, v18\n"
        "v_bfe_u32 v11, v7, 1, 3\n"
        "v_xor_b32 v11, v11, v2\n"
        "v_lshlrev_b32 v11, 4, v11\n"
        "v_lshl_add_u32 v11, v7, 7, v11\n"
        "s_lshl_b32 s65, s48, 12\n"
        "v_add_u32 v14, s65, v11\n"
        "v_mov_b32 v22, 0xc0135761\n"
        "v_mov_b32 v23, 0xc0135761\n"
        "v_mov_b32 v24, 0xbdd2d3e8\n"
        "v_mov_b32 v25, 0xbdd2d3e8\n"
        "s_waitcnt lgkmcnt(0)\n"
        "s_mov_b32 s40, s16\n"
        "s_and_b32 s41, s17, 0xffff\n"
        "s_mov_b32 s32, s8\n"
        "s_and_b32 s33, s9, 0xffff\n"
        "buffer_load_dwordx4 v[32:35], v10, s[32:35], s64 offen offset:0\n"
        "buffer_load_dwordx4 v[36:39], v10, s[32:35], s64 offen offset:32\n"
        "buffer_load_dwordx4 v[40:43], v10, s[32:35], s64 offen offset:64\n"
        "buffer_load_dwordx4 v[44:47], v10, s[32:35], s64 offen offset:96\n"
        "buffer_load_dwordx4 v[48:51], v10, s[32:35], s64 offen offset:128\n"
        "buffer_load_dwordx4 v[52:55], v10, s[32:35], s64 offen offset:160\n"
        "buffer_load_dwordx2 v[26:27], off, s[40:43], 0 sc1\n"
        "s_mul_i32 s65, %1, 5504\n"
        "v_min_u32 v11, 42, v1\n"
        "v_lshlrev_b32 v11, 7, v11\n"
        "v_add_u32 v11, s65, v11\n"
        "global_load_dword v5, v11, s[10:11]\n"
        "s_mov_b32 s36, s12\n"
        "s_and_b32 s37, s13, 0xffff\n"
        "s_mov_b32 s38, 1024\n"
        "s_mov_b32 s39, 0x20000\n"
        "s_mov_b32 s44, s14\n"
        "s_and_b32 s45, s15, 0xffff\n"
        "s_mov_b32 s46, 2097152\n"
        "s_mov_b32 s47, 0x20000\n"
        "s_mov_b32 s72, 0\n"
        "s_mov_b32 s73, 0\n"
        "s_waitcnt vmcnt(2)\n"
        "v_lshlrev_b32 v64, 16, v32\n"
        "v_and_b32 v65, 0xffff0000, v32\n"
        "v_lshlrev_b32 v66, 16, v33\n"
        "v_and_b32 v67, 0xffff0000, v33\n"
        "v_lshlrev_b32 v68, 16, v34\n"
        "v_and_b32 v69, 0xffff0000, v34\n"
        "v_lshlrev_b32 v70, 16, v35\n"
        "v_and_b32 v71, 0xffff0000, v35\n"
        "v_lshlrev_b32 v72, 16, v36\n"
        "v_and_b32 v73, 0xffff0000, v36\n"
        "v_lshlrev_b32 v74, 16, v37\n"
        "v_and_b32 v75, 0xffff0000, v37\n"
        "v_lshlrev_b32 v76, 16, v38\n"
        "v_and_b32 v77, 0xffff0000, v38\n"
        "v_lshlrev_b32 v78, 16, v39\n"
        "v_and_b32 v79, 0xffff0000, v39\n"
        "v_lshlrev_b32 v80, 16, v40\n"
        "v_and_b32 v81, 0xffff0000, v40\n"
        "v_lshlrev_b32 v82, 16, v41\n"
        "v_and_b32 v83, 0xffff0000, v41\n"
        "v_lshlrev_b32 v84, 16, v42\n"
        "v_and_b32 v85, 0xffff0000, v42\n"
        "v_lshlrev_b32 v86, 16, v43\n"
        "v_and_b32 v87, 0xffff0000, v43\n"
        "v_lshlrev_b32 v88, 16, v44\n"
        "v_and_b32 v89, 0xffff0000, v44\n"
        "v_lshlrev_b32 v90, 16, v45\n"
        "v_and_b32 v91, 0xffff0000, v45\n"
        "v_lshlrev_b32 v92, 16, v46\n"
        "v_and_b32 v93, 0xffff0000, v46\n"
        "v_lshlrev_b32 v94, 16, v47\n"
        "v_and_b32 v95, 0xffff0000, v47\n"
        "v_lshlrev_b32 v96, 16, v48\n"
        "v_and_b32 v97, 0xffff0000, v48\n"
        "v_lshlrev_b32 v98, 16, v49\n"
        "v_and_b32 v99, 0xffff0000, v49\n"
        "v_lshlrev_b32 v100, 16, v50\n"
        "v_and_b32 v101, 0xffff0000, v50\n"
        "v_lshlrev_b32 v102, 16, v51\n"
        "v_and_b32 v103, 0xffff0000, v51\n"
        "v_lshlrev_b32 v104, 16, v52\n"
        "v_and_b32 v105, 0xffff0000, v52\n"
        "v_lshlrev_b32 v106, 16, v53\n"
        "v_and_b32 v107, 0xffff0000, v53\n"
        "v_lshlrev_b32 v108, 16, v54\n"
        "v_and_b32 v109, 0xffff0000, v54\n"
        "v_lshlrev_b32 v110, 16, v55\n"
        "v_and_b32 v111, 0xffff0000, v55\n"
        "v_add_u32 v16, s72, v15\n"
        "v_add_u32 v10, s72, v14\n"
        "v_xor_b32 v11, 32, v10\n"
        "v_xor_b32 v12, 64, v10\n"
        "v_xor_b32 v13, 96, v10\n"
        "s_barrier\n"
        "ds_read_b128 v[112:115], v10\n"
        "ds_read_b64_tr_b16 v[128:129], v16 offset:0\n"
        "ds_read_b64_tr_b16 v[130:131], v16 offset:768\n"
        "ds_read_b64_tr_b16 v[132:133], v16 offset:64\n"
        "ds_read_b64_tr_b16 v[134:135], v16 offset:832\n"
        "ds_read_b64_tr_b16 v[136:137], v16 offset:128\n"
        "ds_read_b64_tr_b16 v[138:139], v16 offset:896\n"
        "ds_read_b128 v[116:119], v11\n"
        "ds_read_b64_tr_b16 v[140:141], v16 offset:3072\n"
        "ds_read_b64_tr_b16 v[142:143], v16 offset:3840\n"
        "ds_read_b64_tr_b16 v[144:145], v16 offset:3136\n"
        "ds_read_b64_tr_b16 v[146:147], v16 offset:3904\n"
        "ds_read_b64_tr_b16 v[148:149], v16 offset:3200\n"
        "ds_read_b64_tr_b16 v[150:151], v16 offset:3968\n"
        "Lcloop_%=:\n"
        "s_waitcnt lgkmcnt(7)\n"
        "v_mfma_f32_32x32x16_bf16 v[64:79], v[128:131], v[112:115], v[64:79]\n"
        "ds_read_b128 v[120:123], v12\n"
        "ds_read_b64_tr_b16 v[152:153], v16 offset:6144\n"
        "ds_read_b64_tr_b16 v[154:155], v16 offset:6912\n"
        "v_mfma_f32_32x32x16_bf16 v[80:95], v[132:135], v[112:115], v[80:95]\n"
        "ds_read_b64_tr_b16 v[156:157], v16 offset:6208\n"
        "ds_read_b64_tr_b16 v[158:159], v16 offset:6976\n"
        "v_mfma_f32_32x32x16_bf16 v[96:111], v[136:139], v[112:115], v[96:111]\n"
        "ds_read_b64_tr_b16 v[160:161], v16 offset:6272\n"
        "ds_read_b64_tr_b16 v[162:163], v16 offset:7040\n"
        "s_waitcnt lgkmcnt(7)\n"
        "v_mfma_f32_32x32x16_bf16 v[64:79], v[140:143], v[116:119], v[64:79]\n"
        "ds_read_b128 v[124:127], v13\n"
        "ds_read_b64_tr_b16 v[164:165], v16 offset:9216\n"
        "ds_read_b64_tr_b16 v[166:167], v16 offset:9984\n"
        "v_mfma_f32_32x32x16_bf16 v[80:95], v[144:147], v[116:119], v[80:95]\n"
        "ds_read_b64_tr_b16 v[168:169], v16 offset:9280\n"
        "ds_read_b64_tr_b16 v[170:171], v16 offset:10048\n"
        "v_mfma_f32_32x32x16_bf16 v[96:111], v[148:151], v[116:119], v[96:111]\n"
        "ds_read_b64_tr_b16 v[172:173], v16 offset:9344\n"
        "ds_read_b64_tr_b16 v[174:175], v16 offset:10112\n"
        "s_add_u32 s72, s72, 28672\n"
        "s_cmp_eq_u32 s72, 143360\n"
        "s_cselect_b32 s72, 0, s72\n"
        "s_cmp_eq_u32 s73, 7\n"
        "s_cbranch_scc1 Llastit_%=\n"
        "v_add_u32 v16, s72, v15\n"
        "v_add_u32 v10, s72, v14\n"
        "v_xor_b32 v11, 32, v10\n"
        "v_xor_b32 v12, 64, v10\n"
        "v_xor_b32 v13, 96, v10\n"
        "s_barrier\n"
        "s_waitcnt lgkmcnt(7)\n"
        "v_mfma_f32_32x32x16_bf16 v[64:79], v[152:155], v[120:123], v[64:79]\n"
        "ds_read_b128 v[112:115], v10\n"
        "ds_read_b64_tr_b16 v[128:129], v16 offset:0\n"
        "ds_read_b64_tr_b16 v[130:131], v16 offset:768\n"
        "v_mfma_f32_32x32x16_bf16 v[80:95], v[156:159], v[120:123], v[80:95]\n"
        "ds_read_b64_tr_b16 v[132:133], v16 offset:64\n"
        "ds_read_b64_tr_b16 v[134:135], v16 offset:832\n"
        "v_mfma_f32_32x32x16_bf16 v[96:111], v[160:163], v[120:123], v[96:111]\n"
        "ds_read_b64_tr_b16 v[136:137], v16 offset:128\n"
        "ds_read_b64_tr_b16 v[138:139], v16 offset:896\n"
        "s_waitcnt lgkmcnt(7)\n"
        "v_mfma_f32_32x32x16_bf16 v[64:79], v[164:167], v[124:127], v[64:79]\n"
        "ds_read_b128 v[116:119], v11\n"
        "ds_read_b64_tr_b16 v[140:141], v16 offset:3072\n"
        "ds_read_b64_tr_b16 v[142:143], v16 offset:3840\n"
        "v_mfma_f32_32x32x16_bf16 v[80:95], v[168:171], v[124:127], v[80:95]\n"
        "ds_read_b64_tr_b16 v[144:145], v16 offset:3136\n"
        "ds_read_b64_tr_b16 v[146:147], v16 offset:3904\n"
        "v_mfma_f32_32x32x16_bf16 v[96:111], v[172:175], v[124:127], v[96:111]\n"
        "ds_read_b64_tr_b16 v[148:149], v16 offset:3200\n"
        "ds_read_b64_tr_b16 v[150:151], v16 offset:3968\n"
        "s_cmp_lg_u32 s73, 2\n"
        "s_cbranch_scc1 Lnoack_%=\n"
        "s_waitcnt vmcnt(0)\n"
        "v_readfirstlane_b32 s64, v26\n"
        "v_readfirstlane_b32 s65, v27\n"
        "s_nop 3\n"
        "s_mul_i32 s60, s64, 0x4C957F2D\n"
        "s_mul_hi_u32 s66, s64, 0x4C957F2D\n"
        "s_mul_i32 s67, s64, 0x5851F42D\n"
        "s_add_u32 s66, s66, s67\n"
        "s_mul_i32 s67, s65, 0x4C957F2D\n"
        "s_add_u32 s66, s66, s67\n"
        "s_add_u32 s60, s60, 0xF767814F\n"
        "s_addc_u32 s61, s66, 0x14057B7E\n"
        "v_mov_b32 v26, s60\n"
        "v_mov_b32 v27, s61\n"
        "s_cmp_lg_u32 s48, 0\n"
        "s_cbranch_scc1 Lnoack_%=\n"
        "s_lshl_b32 s64, %1, 3\n"
        "s_add_u32 s64, s64, 4096\n"
        "s_mov_b64 exec, 1\n"
        "buffer_store_dwordx2 v[26:27], off, s[40:43], s64 sc1\n"
        "s_mov_b64 exec, -1\n"
        "Lnoack_%=:\n"
        "s_add_u32 s73, s73, 1\n"
        "s_branch Lcloop_%=\n"
        "Llastit_%=:\n"
        "s_barrier\n"
        "s_mul_i32 s64, s87, 6144\n"
        "s_add_u32 s64, s64, 151552\n"
        "v_add_u32 v2, s64, v17\n"
        "v_add_u32 v3, s64, v18\n"
        "s_waitcnt lgkmcnt(7)\n"
        "v_mfma_f32_32x32x16_bf16 v[64:79], v[152:155], v[120:123], v[64:79]\n"
        "ds_read_b64_tr_b16 v[176:177], v2 offset:0\n"
        "ds_read_b64_tr_b16 v[178:179], v2 offset:512\n"
        "v_mfma_f32_32x32x16_bf16 v[80:95], v[156:159], v[120:123], v[80:95]\n"
        "ds_read_b64_tr_b16 v[180:181], v3 offset:0\n"
        "ds_read_b64_tr_b16 v[182:183], v3 offset:512\n"
        "v_mfma_f32_32x32x16_bf16 v[96:111], v[160:163], v[120:123], v[96:111]\n"
        "ds_read_b64_tr_b16 v[184:185], v2 offset:2048\n"
        "ds_read_b64_tr_b16 v[186:187], v2 offset:2560\n"
        "s_waitcnt lgkmcnt(6)\n"
        "v_mfma_f32_32x32x16_bf16 v[64:79], v[164:167], v[124:127], v[64:79]\n"
        "ds_read_b64_tr_b16 v[188:189], v3 offset:2048\n"
        "ds_read_b64_tr_b16 v[190:191], v3 offset:2560\n"
        "v_mfma_f32_32x32x16_bf16 v[80:95], v[168:171], v[124:127], v[80:95]\n"
        "ds_read_b64_tr_b16 v[192:193], v2 offset:4096\n"
        "ds_read_b64_tr_b16 v[194:195], v2 offset:4608\n"
        "v_mfma_f32_32x32x16_bf16 v[96:111], v[172:175], v[124:127], v[96:111]\n"
        "ds_read_b64_tr_b16 v[196:197], v3 offset:4096\n"
        "ds_read_b64_tr_b16 v[198:199], v3 offset:4608\n"
        "s_waitcnt lgkmcnt(0)\n"
        "s_barrier\n"
        "s_lshl_b32 s89, s50, 2\n"
        "s_add_u32 s89, s89, s48\n"
        "s_mul_i32 s64, s89, 88192\n"
        "s_lshl_b32 s65, s52, 11\n"
        "s_add_u32 s64, s64, s65\n"
        "s_add_u32 s64, s64, 1048576\n"
        "s_nop 3\n"
        "v_pk_mul_f32 v[32:33], v[64:65], v[64:65]\n"
        "v_pk_mul_f32 v[34:35], v[66:67], v[66:67]\n"
        "v_pk_mul_f32 v[36:37], v[68:69], v[68:69]\n"
        "v_pk_mul_f32 v[38:39], v[70:71], v[70:71]\n"
        "v_pk_fma_f32 v[32:33], v[32:33], v[24:25], v[22:23]\n"
        "v_pk_fma_f32 v[34:35], v[34:35], v[24:25], v[22:23]\n"
        "v_pk_fma_f32 v[36:37], v[36:37], v[24:25], v[22:23]\n"
        "v_pk_fma_f32 v[38:39], v[38:39], v[24:25], v[22:23]\n"
        "v_pk_mul_f32 v[32:33], v[32:33], v[64:65]\n"
        "v_pk_mul_f32 v[34:35], v[34:35], v[66:67]\n"
        "v_pk_mul_f32 v[36:37], v[36:37], v[68:69]\n"
        "v_pk_mul_f32 v[38:39], v[38:39], v[70:71]\n"
        "v_exp_f32 v32, v32\n"
        "v_exp_f32 v33, v33\n"
        "v_exp_f32 v34, v34\n"
        "v_exp_f32 v35, v35\n"
        "v_exp_f32 v36, v36\n"
        "v_exp_f32 v37, v37\n"
        "v_exp_f32 v38, v38\n"
        "v_exp_f32 v39, v39\n"
        "v_pk_add_f32 v[32:33], v[32:33], 1.0 op_sel_hi:[1,0]\n"
        "v_pk_add_f32 v[34:35], v[34:35], 1.0 op_sel_hi:[1,0]\n"
        "v_pk_add_f32 v[36:37], v[36:37], 1.0 op_sel_hi:[1,0]\n"
        "v_pk_add_f32 v[38:39], v[38:39], 1.0 op_sel_hi:[1,0]\n"
        "v_rcp_f32 v32, v32\n"
        "v_rcp_f32 v33, v33\n"
        "v_rcp_f32 v34, v34\n"
        "v_rcp_f32 v35, v35\n"
        "v_rcp_f32 v36, v36\n"
        "v_rcp_f32 v37, v37\n"
        "v_rcp_f32 v38, v38\n"
        "v_rcp_f32 v39, v39\n"
        "v_pk_mul_f32 v[32:33], v[32:33], v[64:65]\n"
        "v_pk_mul_f32 v[34:35], v[34:35], v[66:67]\n"
        "v_pk_mul_f32 v[36:37], v[36:37], v[68:69]\n"
        "v_pk_mul_f32 v[38:39], v[38:39], v[70:71]\n"
        "v_cvt_pk_bf16_f32 v40, v32, v33\n"
        "v_cvt_pk_bf16_f32 v41, v34, v35\n"
        "v_cvt_pk_bf16_f32 v42, v36, v37\n"
        "v_cvt_pk_bf16_f32 v43, v38, v39\n"
        "buffer_store_dwordx4 v[40:43], v21, s[40:43], s64 offen sc1\n"
        "s_add_u32 s64, s64, 1024\n"
        "v_pk_mul_f32 v[32:33], v[72:73], v[72:73]\n"
        "v_pk_mul_f32 v[34:35], v[74:75], v[74:75]\n"
        "v_pk_mul_f32 v[36:37], v[76:77], v[76:77]\n"
        "v_pk_mul_f32 v[38:39], v[78:79], v[78:79]\n"
        "v_pk_fma_f32 v[32:33], v[32:33], v[24:25], v[22:23]\n"
        "v_pk_fma_f32 v[34:35], v[34:35], v[24:25], v[22:23]\n"
        "v_pk_fma_f32 v[36:37], v[36:37], v[24:25], v[22:23]\n"
        "v_pk_fma_f32 v[38:39], v[38:39], v[24:25], v[22:23]\n"
        "v_pk_mul_f32 v[32:33], v[32:33], v[72:73]\n"
        "v_pk_mul_f32 v[34:35], v[34:35], v[74:75]\n"
        "v_pk_mul_f32 v[36:37], v[36:37], v[76:77]\n"
        "v_pk_mul_f32 v[38:39], v[38:39], v[78:79]\n"
        "v_exp_f32 v32, v32\n"
        "v_exp_f32 v33, v33\n"
        "v_exp_f32 v34, v34\n"
        "v_exp_f32 v35, v35\n"
        "v_exp_f32 v36, v36\n"
        "v_exp_f32 v37, v37\n"
        "v_exp_f32 v38, v38\n"
        "v_exp_f32 v39, v39\n"
        "v_pk_add_f32 v[32:33], v[32:33], 1.0 op_sel_hi:[1,0]\n"
        "v_pk_add_f32 v[34:35], v[34:35], 1.0 op_sel_hi:[1,0]\n"
        "v_pk_add_f32 v[36:37], v[36:37], 1.0 op_sel_hi:[1,0]\n"
        "v_pk_add_f32 v[38:39], v[38:39], 1.0 op_sel_hi:[1,0]\n"
        "v_rcp_f32 v32, v32\n"
        "v_rcp_f32 v33, v33\n"
        "v_rcp_f32 v34, v34\n"
        "v_rcp_f32 v35, v35\n"
        "v_rcp_f32 v36, v36\n"
        "v_rcp_f32 v37, v37\n"
        "v_rcp_f32 v38, v38\n"
        "v_rcp_f32 v39, v39\n"
        "v_pk_mul_f32 v[32:33], v[32:33], v[72:73]\n"
        "v_pk_mul_f32 v[34:35], v[34:35], v[74:75]\n"
        "v_pk_mul_f32 v[36:37], v[36:37], v[76:77]\n"
        "v_pk_mul_f32 v[38:39], v[38:39], v[78:79]\n"
        "v_cvt_pk_bf16_f32 v44, v32, v33\n"
        "v_cvt_pk_bf16_f32 v45, v34, v35\n"
        "v_cvt_pk_bf16_f32 v46, v36, v37\n"
        "v_cvt_pk_bf16_f32 v47, v38, v39\n"
        "buffer_store_dwordx4 v[44:47], v21, s[40:43], s64 offen sc1\n"
        "s_add_u32 s64, s64, 1024\n"
        "v_pk_mul_f32 v[32:33], v[80:81], v[80:81]\n"
        "v_pk_mul_f32 v[34:35], v[82:83], v[82:83]\n"
        "v_pk_mul_f32 v[36:37], v[84:85], v[84:85]\n"
        "v_pk_mul_f32 v[38:39], v[86:87], v[86:87]\n"
        "v_pk_fma_f32 v[32:33], v[32:33], v[24:25], v[22:23]\n"
        "v_pk_fma_f32 v[34:35], v[34:35], v[24:25], v[22:23]\n"
        "v_pk_fma_f32 v[36:37], v[36:37], v[24:25], v[22:23]\n"
        "v_pk_fma_f32 v[38:39], v[38:39], v[24:25], v[22:23]\n"
        "v_pk_mul_f32 v[32:33], v[32:33], v[80:81]\n"
        "v_pk_mul_f32 v[34:35], v[34:35], v[82:83]\n"
        "v_pk_mul_f32 v[36:37], v[36:37], v[84:85]\n"
        "v_pk_mul_f32 v[38:39], v[38:39], v[86:87]\n"
        "v_exp_f32 v32, v32\n"
        "v_exp_f32 v33, v33\n"
        "v_exp_f32 v34, v34\n"
        "v_exp_f32 v35, v35\n"
        "v_exp_f32 v36, v36\n"
        "v_exp_f32 v37, v37\n"
        "v_exp_f32 v38, v38\n"
        "v_exp_f32 v39, v39\n"
        "v_pk_add_f32 v[32:33], v[32:33], 1.0 op_sel_hi:[1,0]\n"
        "v_pk_add_f32 v[34:35], v[34:35], 1.0 op_sel_hi:[1,0]\n"
        "v_pk_add_f32 v[36:37], v[36:37], 1.0 op_sel_hi:[1,0]\n"
        "v_pk_add_f32 v[38:39], v[38:39], 1.0 op_sel_hi:[1,0]\n"
        "v_rcp_f32 v32, v32\n"
        "v_rcp_f32 v33, v33\n"
        "v_rcp_f32 v34, v34\n"
        "v_rcp_f32 v35, v35\n"
        "v_rcp_f32 v36, v36\n"
        "v_rcp_f32 v37, v37\n"
        "v_rcp_f32 v38, v38\n"
        "v_rcp_f32 v39, v39\n"
        "v_pk_mul_f32 v[32:33], v[32:33], v[80:81]\n"
        "v_pk_mul_f32 v[34:35], v[34:35], v[82:83]\n"
        "v_pk_mul_f32 v[36:37], v[36:37], v[84:85]\n"
        "v_pk_mul_f32 v[38:39], v[38:39], v[86:87]\n"
        "v_cvt_pk_bf16_f32 v40, v32, v33\n"
        "v_cvt_pk_bf16_f32 v41, v34, v35\n"
        "v_cvt_pk_bf16_f32 v42, v36, v37\n"
        "v_cvt_pk_bf16_f32 v43, v38, v39\n"
        "buffer_store_dwordx4 v[40:43], v21, s[40:43], s64 offen sc1\n"
        "s_add_u32 s64, s64, 1024\n"
        "v_pk_mul_f32 v[32:33], v[88:89], v[88:89]\n"
        "v_pk_mul_f32 v[34:35], v[90:91], v[90:91]\n"
        "v_pk_mul_f32 v[36:37], v[92:93], v[92:93]\n"
        "v_pk_mul_f32 v[38:39], v[94:95], v[94:95]\n"
        "v_pk_fma_f32 v[32:33], v[32:33], v[24:25], v[22:23]\n"
        "v_pk_fma_f32 v[34:35], v[34:35], v[24:25], v[22:23]\n"
        "v_pk_fma_f32 v[36:37], v[36:37], v[24:25], v[22:23]\n"
        "v_pk_fma_f32 v[38:39], v[38:39], v[24:25], v[22:23]\n"
        "v_pk_mul_f32 v[32:33], v[32:33], v[88:89]\n"
        "v_pk_mul_f32 v[34:35], v[34:35], v[90:91]\n"
        "v_pk_mul_f32 v[36:37], v[36:37], v[92:93]\n"
        "v_pk_mul_f32 v[38:39], v[38:39], v[94:95]\n"
        "v_exp_f32 v32, v32\n"
        "v_exp_f32 v33, v33\n"
        "v_exp_f32 v34, v34\n"
        "v_exp_f32 v35, v35\n"
        "v_exp_f32 v36, v36\n"
        "v_exp_f32 v37, v37\n"
        "v_exp_f32 v38, v38\n"
        "v_exp_f32 v39, v39\n"
        "v_pk_add_f32 v[32:33], v[32:33], 1.0 op_sel_hi:[1,0]\n"
        "v_pk_add_f32 v[34:35], v[34:35], 1.0 op_sel_hi:[1,0]\n"
        "v_pk_add_f32 v[36:37], v[36:37], 1.0 op_sel_hi:[1,0]\n"
        "v_pk_add_f32 v[38:39], v[38:39], 1.0 op_sel_hi:[1,0]\n"
        "v_rcp_f32 v32, v32\n"
        "v_rcp_f32 v33, v33\n"
        "v_rcp_f32 v34, v34\n"
        "v_rcp_f32 v35, v35\n"
        "v_rcp_f32 v36, v36\n"
        "v_rcp_f32 v37, v37\n"
        "v_rcp_f32 v38, v38\n"
        "v_rcp_f32 v39, v39\n"
        "v_pk_mul_f32 v[32:33], v[32:33], v[88:89]\n"
        "v_pk_mul_f32 v[34:35], v[34:35], v[90:91]\n"
        "v_pk_mul_f32 v[36:37], v[36:37], v[92:93]\n"
        "v_pk_mul_f32 v[38:39], v[38:39], v[94:95]\n"
        "v_cvt_pk_bf16_f32 v44, v32, v33\n"
        "v_cvt_pk_bf16_f32 v45, v34, v35\n"
        "v_cvt_pk_bf16_f32 v46, v36, v37\n"
        "v_cvt_pk_bf16_f32 v47, v38, v39\n"
        "buffer_store_dwordx4 v[44:47], v21, s[40:43], s64 offen sc1\n"
        "s_add_u32 s64, s64, 1024\n"
        "s_cmp_lt_u32 s53, 3\n"
        "s_cbranch_scc1 Lskipu2_%=\n"
        "v_pk_mul_f32 v[32:33], v[96:97], v[96:97]\n"
        "v_pk_mul_f32 v[34:35], v[98:99], v[98:99]\n"
        "v_pk_mul_f32 v[36:37], v[100:101], v[100:101]\n"
        "v_pk_mul_f32 v[38:39], v[102:103], v[102:103]\n"
        "v_pk_fma_f32 v[32:33], v[32:33], v[24:25], v[22:23]\n"
        "v_pk_fma_f32 v[34:35], v[34:35], v[24:25], v[22:23]\n"
        "v_pk_fma_f32 v[36:37], v[36:37], v[24:25], v[22:23]\n"
        "v_pk_fma_f32 v[38:39], v[38:39], v[24:25], v[22:23]\n"
        "v_pk_mul_f32 v[32:33], v[32:33], v[96:97]\n"
        "v_pk_mul_f32 v[34:35], v[34:35], v[98:99]\n"
        "v_pk_mul_f32 v[36:37], v[36:37], v[100:101]\n"
        "v_pk_mul_f32 v[38:39], v[38:39], v[102:103]\n"
        "v_exp_f32 v32, v32\n"
        "v_exp_f32 v33, v33\n"
        "v_exp_f32 v34, v34\n"
        "v_exp_f32 v35, v35\n"
        "v_exp_f32 v36, v36\n"
        "v_exp_f32 v37, v37\n"
        "v_exp_f32 v38, v38\n"
        "v_exp_f32 v39, v39\n"
        "v_pk_add_f32 v[32:33], v[32:33], 1.0 op_sel_hi:[1,0]\n"
        "v_pk_add_f32 v[34:35], v[34:35], 1.0 op_sel_hi:[1,0]\n"
        "v_pk_add_f32 v[36:37], v[36:37], 1.0 op_sel_hi:[1,0]\n"
        "v_pk_add_f32 v[38:39], v[38:39], 1.0 op_sel_hi:[1,0]\n"
        "v_rcp_f32 v32, v32\n"
        "v_rcp_f32 v33, v33\n"
        "v_rcp_f32 v34, v34\n"
        "v_rcp_f32 v35, v35\n"
        "v_rcp_f32 v36, v36\n"
        "v_rcp_f32 v37, v37\n"
        "v_rcp_f32 v38, v38\n"
        "v_rcp_f32 v39, v39\n"
        "v_pk_mul_f32 v[32:33], v[32:33], v[96:97]\n"
        "v_pk_mul_f32 v[34:35], v[34:35], v[98:99]\n"
        "v_pk_mul_f32 v[36:37], v[36:37], v[100:101]\n"
        "v_pk_mul_f32 v[38:39], v[38:39], v[102:103]\n"
        "v_cvt_pk_bf16_f32 v40, v32, v33\n"
        "v_cvt_pk_bf16_f32 v41, v34, v35\n"
        "v_cvt_pk_bf16_f32 v42, v36, v37\n"
        "v_cvt_pk_bf16_f32 v43, v38, v39\n"
        "buffer_store_dwordx4 v[40:43], v21, s[40:43], s64 offen sc1\n"
        "s_add_u32 s64, s64, 1024\n"
        "v_pk_mul_f32 v[32:33], v[104:105], v[104:105]\n"
        "v_pk_mul_f32 v[34:35], v[106:107], v[106:107]\n"
        "v_pk_mul_f32 v[36:37], v[108:109], v[108:109]\n"
        "v_pk_mul_f32 v[38:39], v[110:111], v[110:111]\n"
        "v_pk_fma_f32 v[32:33], v[32:33], v[24:25], v[22:23]\n"
        "v_pk_fma_f32 v[34:35], v[34:35], v[24:25], v[22:23]\n"
        "v_pk_fma_f32 v[36:37], v[36:37], v[24:25], v[22:23]\n"
        "v_pk_fma_f32 v[38:39], v[38:39], v[24:25], v[22:23]\n"
        "v_pk_mul_f32 v[32:33], v[32:33], v[104:105]\n"
        "v_pk_mul_f32 v[34:35], v[34:35], v[106:107]\n"
        "v_pk_mul_f32 v[36:37], v[36:37], v[108:109]\n"
        "v_pk_mul_f32 v[38:39], v[38:39], v[110:111]\n"
        "v_exp_f32 v32, v32\n"
        "v_exp_f32 v33, v33\n"
        "v_exp_f32 v34, v34\n"
        "v_exp_f32 v35, v35\n"
        "v_exp_f32 v36, v36\n"
        "v_exp_f32 v37, v37\n"
        "v_exp_f32 v38, v38\n"
        "v_exp_f32 v39, v39\n"
        "v_pk_add_f32 v[32:33], v[32:33], 1.0 op_sel_hi:[1,0]\n"
        "v_pk_add_f32 v[34:35], v[34:35], 1.0 op_sel_hi:[1,0]\n"
        "v_pk_add_f32 v[36:37], v[36:37], 1.0 op_sel_hi:[1,0]\n"
        "v_pk_add_f32 v[38:39], v[38:39], 1.0 op_sel_hi:[1,0]\n"
        "v_rcp_f32 v32, v32\n"
        "v_rcp_f32 v33, v33\n"
        "v_rcp_f32 v34, v34\n"
        "v_rcp_f32 v35, v35\n"
        "v_rcp_f32 v36, v36\n"
        "v_rcp_f32 v37, v37\n"
        "v_rcp_f32 v38, v38\n"
        "v_rcp_f32 v39, v39\n"
        "v_pk_mul_f32 v[32:33], v[32:33], v[104:105]\n"
        "v_pk_mul_f32 v[34:35], v[34:35], v[106:107]\n"
        "v_pk_mul_f32 v[36:37], v[36:37], v[108:109]\n"
        "v_pk_mul_f32 v[38:39], v[38:39], v[110:111]\n"
        "v_cvt_pk_bf16_f32 v44, v32, v33\n"
        "v_cvt_pk_bf16_f32 v45, v34, v35\n"
        "v_cvt_pk_bf16_f32 v46, v36, v37\n"
        "v_cvt_pk_bf16_f32 v47, v38, v39\n"
        "buffer_store_dwordx4 v[44:47], v21, s[40:43], s64 offen sc1\n"
        "s_add_u32 s64, s64, 1024\n"
        "Lskipu2_%=:\n"
        "s_lshl_b32 s68, s89, 4\n"
        "s_add_u32 s68, s68, s51\n"
        "s_lshl_b32 s68, s68, 7\n"
        "s_add_u32 s68, s68, 8192\n"
        "s_lshl_b32 s89, s55, 1\n"
        "s_add_u32 s89, s89, s88\n"
        "s_add_u32 s64, s87, 7\n"
        "v_min_u32 v3, s64, v1\n"
        "s_mul_i32 s65, s87, 7\n"
        "s_lshl_b32 s66, s89, 4\n"
        "s_add_u32 s65, s65, s66\n"
        "v_add_u32 v3, s65, v3\n"
        "v_lshlrev_b32 v3, 7, v3\n"
        "v_add_u32 v3, 8192, v3\n"
        "s_mul_i32 s85, s89, 88192\n"
        "s_mul_i32 s64, s87, 44032\n"
        "s_add_u32 s85, s85, s64\n"
        "s_add_u32 s85, s85, 1048576\n"
        "s_add_u32 s86, s85, 44032\n"
        "s_lshl_b32 s64, s57, 10\n"
        "s_add_u32 s84, s85, s64\n"
        "s_mul_i32 s64, s87, 81920\n"
        "v_add_u32 v17, s64, v17\n"
        "v_add_u32 v18, s64, v18\n"
        "v_add_u32 v19, 65536, v17\n"
        "v_add_u32 v20, 65536, v18\n"
        "s_mov_b32 s90, 200000\n"
        "v_lshrrev_b32 v2, 5, v1\n"
        "v_lshlrev_b32 v2, 4, v2\n"
        "s_lshl_b32 s64, s56, 7\n"
        "s_lshl_b32 s65, s87, 6\n"
        "s_add_u32 s64, s64, s65\n"
        "s_waitcnt vmcnt(0)\n"
        "s_mov_b64 exec, 1\n"
        "buffer_store_dwordx2 v[26:27], off, s[40:43], s68 sc1\n"
        "s_mov_b64 exec, -1\n"
        "buffer_load_dwordx4 v[32:35], v2, s[36:39], s64 offen\n"
        "buffer_load_dwordx4 v[36:39], v2, s[36:39], s64 offen offset:32\n"
        "Lpoll_%=:\n"
        "buffer_load_dwordx2 v[4:5], v3, s[40:43], 0 offen sc1\n"
        "s_waitcnt vmcnt(0)\n"
        "v_cmp_eq_u32 vcc, s60, v4\n"
        "v_cmp_eq_u32 s[82:83], s61, v5\n"
        "s_nop 1\n"
        "s_and_b64 vcc, vcc, s[82:83]\n"
        "s_cmp_eq_u64 vcc, -1\n"
        "s_cbranch_scc1 Lpolled_%=\n"
        "s_sub_u32 s90, s90, 1\n"
        "s_cbranch_scc1 Lpolled_%=\n"
        "s_sleep 1\n"
        "s_branch Lpoll_%=\n"
        "Lpolled_%=:\n"
        "buffer_load_dwordx4 v[200:203], v21, s[40:43], s84 offen sc1\n"
        "s_add_u32 s84, s84, 1024\n"
        "s_cmp_eq_u32 s84, s86\n"
        "s_cselect_b32 s84, s85, s84\n"
        "buffer_load_dwordx4 v[204:207], v21, s[40:43], s84 offen sc1\n"
        "s_add_u32 s84, s84, 1024\n"
        "s_cmp_eq_u32 s84, s86\n"
        "s_cselect_b32 s84, s85, s84\n"
        "buffer_load_dwordx4 v[208:211], v21, s[40:43], s84 offen sc1\n"
        "s_add_u32 s84, s84, 1024\n"
        "s_cmp_eq_u32 s84, s86\n"
        "s_cselect_b32 s84, s85, s84\n"
        "buffer_load_dwordx4 v[212:215], v21, s[40:43], s84 offen sc1\n"
        "s_add_u32 s84, s84, 1024\n"
        "s_cmp_eq_u32 s84, s86\n"
        "s_cselect_b32 s84, s85, s84\n"
        "buffer_load_dwordx4 v[216:219], v21, s[40:43], s84 offen sc1\n"
        "s_add_u32 s84, s84, 1024\n"
        "s_cmp_eq_u32 s84, s86\n"
        "s_cselect_b32 s84, s85, s84\n"
        "buffer_load_dwordx4 v[220:223], v21, s[40:43], s84 offen sc1\n"
        "s_add_u32 s84, s84, 1024\n"
        "s_cmp_eq_u32 s84, s86\n"
        "s_cselect_b32 s84, s85, s84\n"
        "buffer_load_dwordx4 v[224:227], v21, s[40:43], s84 offen sc1\n"
        "s_add_u32 s84, s84, 1024\n"
        "s_cmp_eq_u32 s84, s86\n"
        "s_cselect_b32 s84, s85, s84\n"
        "buffer_load_dwordx4 v[228:231], v21, s[40:43], s84 offen sc1\n"
        "s_add_u32 s84, s84, 1024\n"
        "s_cmp_eq_u32 s84, s86\n"
        "s_cselect_b32 s84, s85, s84\n"
        "buffer_load_dwordx4 v[232:235], v21, s[40:43], s84 offen sc1\n"
        "s_add_u32 s84, s84, 1024\n"
        "s_cmp_eq_u32 s84, s86\n"
        "s_cselect_b32 s84, s85, s84\n"
        "buffer_load_dwordx4 v[236:239], v21, s[40:43], s84 offen sc1\n"
        "s_add_u32 s84, s84, 1024\n"
        "s_cmp_eq_u32 s84, s86\n"
        "s_cselect_b32 s84, s85, s84\n"
        "buffer_load_dwordx4 v[240:243], v21, s[40:43], s84 offen sc1\n"
        "s_add_u32 s84, s84, 1024\n"
        "s_cmp_eq_u32 s84, s86\n"
        "s_cselect_b32 s84, s85, s84\n"
        "buffer_load_dwordx4 v[244:247], v21, s[40:43], s84 offen sc1\n"
        "s_add_u32 s84, s84, 1024\n"
        "s_cmp_eq_u32 s84, s86\n"
        "s_cselect_b32 s84, s85, s84\n"
        "v_lshlrev_b32 v80, 16, v32\n"
        "v_and_b32 v81, 0xffff0000, v32\n"
        "v_lshlrev_b32 v82, 16, v33\n"
        "v_and_b32 v83, 0xffff0000, v33\n"
        "v_lshlrev_b32 v84, 16, v34\n"
        "v_and_b32 v85, 0xffff0000, v34\n"
        "v_lshlrev_b32 v86, 16, v35\n"
        "v_and_b32 v87, 0xffff0000, v35\n"
        "v_lshlrev_b32 v88, 16, v36\n"
        "v_and_b32 v89, 0xffff0000, v36\n"
        "v_lshlrev_b32 v90, 16, v37\n"
        "v_and_b32 v91, 0xffff0000, v37\n"
        "v_lshlrev_b32 v92, 16, v38\n"
        "v_and_b32 v93, 0xffff0000, v38\n"
        "v_lshlrev_b32 v94, 16, v39\n"
        "v_and_b32 v95, 0xffff0000, v39\n"
        "v_mov_b32 v64, 0\n"
        "v_mov_b32 v65, 0\n"
        "v_mov_b32 v66, 0\n"
        "v_mov_b32 v67, 0\n"
        "v_mov_b32 v68, 0\n"
        "v_mov_b32 v69, 0\n"
        "v_mov_b32 v70, 0\n"
        "v_mov_b32 v71, 0\n"
        "v_mov_b32 v72, 0\n"
        "v_mov_b32 v73, 0\n"
        "v_mov_b32 v74, 0\n"
        "v_mov_b32 v75, 0\n"
        "v_mov_b32 v76, 0\n"
        "v_mov_b32 v77, 0\n"
        "v_mov_b32 v78, 0\n"
        "v_mov_b32 v79, 0\n"
        "s_barrier\n"
        "s_waitcnt vmcnt(11)\n"
        "v_mfma_f32_32x32x16_bf16 v[64:79], v[176:179], v[200:203], v[64:79]\n"
        "v_mfma_f32_32x32x16_bf16 v[80:95], v[180:183], v[200:203], v[80:95]\n"
        "buffer_load_dwordx4 v[200:203], v21, s[40:43], s84 offen sc1\n"
        "s_waitcnt vmcnt(11)\n"
        "v_mfma_f32_32x32x16_bf16 v[64:79], v[184:187], v[204:207], v[64:79]\n"
        "ds_read_b64_tr_b16 v[112:113], v17 offset:0\n"
        "ds_read_b64_tr_b16 v[114:115], v17 offset:512\n"
        "s_add_u32 s84, s84, 1024\n"
        "s_cmp_eq_u32 s84, s86\n"
        "s_cselect_b32 s84, s85, s84\n"
        "v_mfma_f32_32x32x16_bf16 v[80:95], v[188:191], v[204:207], v[80:95]\n"
        "buffer_load_dwordx4 v[204:207], v21, s[40:43], s84 offen sc1\n"
        "ds_read_b64_tr_b16 v[116:117], v18 offset:0\n"
        "ds_read_b64_tr_b16 v[118:119], v18 offset:512\n"
        "s_waitcnt vmcnt(11)\n"
        "v_mfma_f32_32x32x16_bf16 v[64:79], v[192:195], v[208:211], v[64:79]\n"
        "ds_read_b64_tr_b16 v[120:121], v17 offset:2048\n"
        "ds_read_b64_tr_b16 v[122:123], v17 offset:2560\n"
        "s_add_u32 s84, s84, 1024\n"
        "s_cmp_eq_u32 s84, s86\n"
        "s_cselect_b32 s84, s85, s84\n"
        "v_mfma_f32_32x32x16_bf16 v[80:95], v[196:199], v[208:211], v[80:95]\n"
        "buffer_load_dwordx4 v[208:211], v21, s[40:43], s84 offen sc1\n"
        "ds_read_b64_tr_b16 v[124:125], v18 offset:2048\n"
        "ds_read_b64_tr_b16 v[126:127], v18 offset:2560\n"
        "s_waitcnt vmcnt(11) lgkmcnt(4)\n"
        "v_mfma_f32_32x32x16_bf16 v[64:79], v[112:115], v[212:215], v[64:79]\n"
        "ds_read_b64_tr_b16 v[128:129], v17 offset:4096\n"
        "ds_read_b64_tr_b16 v[130:131], v17 offset:4608\n"
        "s_add_u32 s84, s84, 1024\n"
        "s_cmp_eq_u32 s84, s86\n"
        "s_cselect_b32 s84, s85, s84\n"
        "v_mfma_f32_32x32x16_bf16 v[80:95], v[116:119], v[212:215], v[80:95]\n"
        "buffer_load_dwordx4 v[212:215], v21, s[40:43], s84 offen sc1\n"
        "ds_read_b64_tr_b16 v[132:133], v18 offset:4096\n"
        "ds_read_b64_tr_b16 v[134:135], v18 offset:4608\n"
        "s_waitcnt vmcnt(11) lgkmcnt(4)\n"
        "v_mfma_f32_32x32x16_bf16 v[64:79], v[120:123], v[216:219], v[64:79]\n"
        "ds_read_b64_tr_b16 v[112:113], v17 offset:6144\n"
        "ds_read_b64_tr_b16 v[114:115], v17 offset:6656\n"
        "s_add_u32 s84, s84, 1024\n"
        "s_cmp_eq_u32 s84, s86\n"
        "s_cselect_b32 s84, s85, s84\n"
        "v_mfma_f32_32x32x16_bf16 v[80:95], v[124:127], v[216:219], v[80:95]\n"
        "buffer_load_dwordx4 v[216:219], v21, s[40:43], s84 offen sc1\n"
        "ds_read_b64_tr_b16 v[116:117], v18 offset:6144\n"
        "ds_read_b64_tr_b16 v[118:119], v18 offset:6656\n"
        "s_waitcnt vmcnt(11) lgkmcnt(4)\n"
        "v_mfma_f32_32x32x16_bf16 v[64:79], v[128:131], v[220:223], v[64:79]\n"
        "ds_read_b64_tr_b16 v[120:121], v17 offset:8192\n"
        "ds_read_b64_tr_b16 v[122:123], v17 offset:8704\n"
        "s_add_u32 s84, s84, 1024\n"
        "s_cmp_eq_u32 s84, s86\n"
        "s_cselect_b32 s84, s85, s84\n"
        "v_mfma_f32_32x32x16_bf16 v[80:95], v[132:135], v[220:223], v[80:95]\n"
        "buffer_load_dwordx4 v[220:223], v21, s[40:43], s84 offen sc1\n"
        "ds_read_b64_tr_b16 v[124:125], v18 offset:8192\n"
        "ds_read_b64_tr_b16 v[126:127], v18 offset:8704\n"
        "s_waitcnt vmcnt(11) lgkmcnt(4)\n"
        "v_mfma_f32_32x32x16_bf16 v[64:79], v[112:115], v[224:227], v[64:79]\n"
        "ds_read_b64_tr_b16 v[128:129], v17 offset:10240\n"
        "ds_read_b64_tr_b16 v[130:131], v17 offset:10752\n"
        "s_add_u32 s84, s84, 1024\n"
        "s_cmp_eq_u32 s84, s86\n"
        "s_cselect_b32 s84, s85, s84\n"
        "v_mfma_f32_32x32x16_bf16 v[80:95], v[116:119], v[224:227], v[80:95]\n"
        "buffer_load_dwordx4 v[224:227], v21, s[40:43], s84 offen sc1\n"
        "ds_read_b64_tr_b16 v[132:133], v18 offset:10240\n"
        "ds_read_b64_tr_b16 v[134:135], v18 offset:10752\n"
        "s_waitcnt vmcnt(11) lgkmcnt(4)\n"
        "v_mfma_f32_32x32x16_bf16 v[64:79], v[120:123], v[228:231], v[64:79]\n"
        "ds_read_b64_tr_b16 v[112:113], v17 offset:12288\n"
        "ds_read_b64_tr_b16 v[114:115], v17 offset:12800\n"
        "s_add_u32 s84, s84, 1024\n"
        "s_cmp_eq_u32 s84, s86\n"
        "s_cselect_b32 s84, s85, s84\n"
        "v_mfma_f32_32x32x16_bf16 v[80:95], v[124:127], v[228:231], v[80:95]\n"
        "buffer_load_dwordx4 v[228:231], v21, s[40:43], s84 offen sc1\n"
        "ds_read_b64_tr_b16 v[116:117], v18 offset:12288\n"
        "ds_read_b64_tr_b16 v[118:119], v18 offset:12800\n"
        "s_waitcnt vmcnt(11) lgkmcnt(4)\n"
        "v_mfma_f32_32x32x16_bf16 v[64:79], v[128:131], v[232:235], v[64:79]\n"
        "ds_read_b64_tr_b16 v[120:121], v17 offset:14336\n"
        "ds_read_b64_tr_b16 v[122:123], v17 offset:14848\n"
        "s_add_u32 s84, s84, 1024\n"
        "s_cmp_eq_u32 s84, s86\n"
        "s_cselect_b32 s84, s85, s84\n"
        "v_mfma_f32_32x32x16_bf16 v[80:95], v[132:135], v[232:235], v[80:95]\n"
        "buffer_load_dwordx4 v[232:235], v21, s[40:43], s84 offen sc1\n"
        "ds_read_b64_tr_b16 v[124:125], v18 offset:14336\n"
        "ds_read_b64_tr_b16 v[126:127], v18 offset:14848\n"
        "s_waitcnt vmcnt(11) lgkmcnt(4)\n"
        "v_mfma_f32_32x32x16_bf16 v[64:79], v[112:115], v[236:239], v[64:79]\n"
        "ds_read_b64_tr_b16 v[128:129], v17 offset:16384\n"
        "ds_read_b64_tr_b16 v[130:131], v17 offset:16896\n"
        "s_add_u32 s84, s84, 1024\n"
        "s_cmp_eq_u32 s84, s86\n"
        "s_cselect_b32 s84, s85, s84\n"
        "v_mfma_f32_32x32x16_bf16 v[80:95], v[116:119], v[236:239], v[80:95]\n"
        "buffer_load_dwordx4 v[236:239], v21, s[40:43], s84 offen sc1\n"
        "ds_read_b64_tr_b16 v[132:133], v18 offset:16384\n"
        "ds_read_b64_tr_b16 v[134:135], v18 offset:16896\n"
        "s_waitcnt vmcnt(11) lgkmcnt(4)\n"
        "v_mfma_f32_32x32x16_bf16 v[64:79], v[120:123], v[240:243], v[64:79]\n"
        "ds_read_b64_tr_b16 v[112:113], v17 offset:18432\n"
        "ds_read_b64_tr_b16 v[114:115], v17 offset:18944\n"
        "s_add_u32 s84, s84, 1024\n"
        "s_cmp_eq_u32 s84, s86\n"
        "s_cselect_b32 s84, s85, s84\n"
        "v_mfma_f32_32x32x16_bf16 v[80:95], v[124:127], v[240:243], v[80:95]\n"
        "buffer_load_dwordx4 v[240:243], v21, s[40:43], s84 offen sc1\n"
        "ds_read_b64_tr_b16 v[116:117], v18 offset:18432\n"
        "ds_read_b64_tr_b16 v[118:119], v18 offset:18944\n"
        "s_waitcnt vmcnt(11) lgkmcnt(4)\n"
        "v_mfma_f32_32x32x16_bf16 v[64:79], v[128:131], v[244:247], v[64:79]\n"
        "ds_read_b64_tr_b16 v[120:121], v17 offset:20480\n"
        "ds_read_b64_tr_b16 v[122:123], v17 offset:20992\n"
        "s_add_u32 s84, s84, 1024\n"
        "s_cmp_eq_u32 s84, s86\n"
        "s_cselect_b32 s84, s85, s84\n"
        "v_mfma_f32_32x32x16_bf16 v[80:95], v[132:135], v[244:247], v[80:95]\n"
        "buffer_load_dwordx4 v[244:247], v21, s[40:43], s84 offen sc1\n"
        "ds_read_b64_tr_b16 v[124:125], v18 offset:20480\n"
        "ds_read_b64_tr_b16 v[126:127], v18 offset:20992\n"
        "s_waitcnt vmcnt(11) lgkmcnt(4)\n"
        "v_mfma_f32_32x32x16_bf16 v[64:79], v[112:115], v[200:203], v[64:79]\n"
        "ds_read_b64_tr_b16 v[128:129], v17 offset:22528\n"
        "ds_read_b64_tr_b16 v[130:131], v17 offset:23040\n"
        "s_add_u32 s84, s84, 1024\n"
        "s_cmp_eq_u32 s84, s86\n"
        "s_cselect_b32 s84, s85, s84\n"
        "v_mfma_f32_32x32x16_bf16 v[80:95], v[116:119], v[200:203], v[80:95]\n"
        "buffer_load_dwordx4 v[200:203], v21, s[40:43], s84 offen sc1\n"
        "ds_read_b64_tr_b16 v[132:133], v18 offset:22528\n"
        "ds_read_b64_tr_b16 v[134:135], v18 offset:23040\n"
        "s_waitcnt vmcnt(11) lgkmcnt(4)\n"
        "v_mfma_f32_32x32x16_bf16 v[64:79], v[120:123], v[204:207], v[64:79]\n"
        "ds_read_b64_tr_b16 v[112:113], v17 offset:24576\n"
        "ds_read_b64_tr_b16 v[114:115], v17 offset:25088\n"
        "s_add_u32 s84, s84, 1024\n"
        "s_cmp_eq_u32 s84, s86\n"
        "s_cselect_b32 s84, s85, s84\n"
        "v_mfma_f32_32x32x16_bf16 v[80:95], v[124:127], v[204:207], v[80:95]\n"
        "buffer_load_dwordx4 v[204:207], v21, s[40:43], s84 offen sc1\n"
        "ds_read_b64_tr_b16 v[116:117], v18 offset:24576\n"
        "ds_read_b64_tr_b16 v[118:119], v18 offset:25088\n"
        "s_waitcnt vmcnt(11) lgkmcnt(4)\n"
        "v_mfma_f32_32x32x16_bf16 v[64:79], v[128:131], v[208:211], v[64:79]\n"
        "ds_read_b64_tr_b16 v[120:121], v17 offset:26624\n"
        "ds_read_b64_tr_b16 v[122:123], v17 offset:27136\n"
        "s_add_u32 s84, s84, 1024\n"
        "s_cmp_eq_u32 s84, s86\n"
        "s_cselect_b32 s84, s85, s84\n"
        "v_mfma_f32_32x32x16_bf16 v[80:95], v[132:135], v[208:211], v[80:95]\n"
        "buffer_load_dwordx4 v[208:211], v21, s[40:43], s84 offen sc1\n"
        "ds_read_b64_tr_b16 v[124:125], v18 offset:26624\n"
        "ds_read_b64_tr_b16 v[126:127], v18 offset:27136\n"
        "s_waitcnt vmcnt(11) lgkmcnt(4)\n"
        "v_mfma_f32_32x32x16_bf16 v[64:79], v[112:115], v[212:215], v[64:79]\n"
        "ds_read_b64_tr_b16 v[128:129], v17 offset:28672\n"
        "ds_read_b64_tr_b16 v[130:131], v17 offset:29184\n"
        "s_add_u32 s84, s84, 1024\n"
        "s_cmp_eq_u32 s84, s86\n"
        "s_cselect_b32 s84, s85, s84\n"
        "v_mfma_f32_32x32x16_bf16 v[80:95], v[116:119], v[212:215], v[80:95]\n"
        "buffer_load_dwordx4 v[212:215], v21, s[40:43], s84 offen sc1\n"
        "ds_read_b64_tr_b16 v[132:133], v18 offset:28672\n"
        "ds_read_b64_tr_b16 v[134:135], v18 offset:29184\n"
        "s_waitcnt vmcnt(11) lgkmcnt(4)\n"
        "v_mfma_f32_32x32x16_bf16 v[64:79], v[120:123], v[216:219], v[64:79]\n"
        "ds_read_b64_tr_b16 v[112:113], v17 offset:30720\n"
        "ds_read_b64_tr_b16 v[114:115], v17 offset:31232\n"
        "s_add_u32 s84, s84, 1024\n"
        "s_cmp_eq_u32 s84, s86\n"
        "s_cselect_b32 s84, s85, s84\n"
        "v_mfma_f32_32x32x16_bf16 v[80:95], v[124:127], v[216:219], v[80:95]\n"
        "buffer_load_dwordx4 v[216:219], v21, s[40:43], s84 offen sc1\n"
        "ds_read_b64_tr_b16 v[116:117], v18 offset:30720\n"
        "ds_read_b64_tr_b16 v[118:119], v18 offset:31232\n"
        "s_waitcnt vmcnt(11) lgkmcnt(4)\n"
        "v_mfma_f32_32x32x16_bf16 v[64:79], v[128:131], v[220:223], v[64:79]\n"
        "ds_read_b64_tr_b16 v[120:121], v17 offset:32768\n"
        "ds_read_b64_tr_b16 v[122:123], v17 offset:33280\n"
        "s_add_u32 s84, s84, 1024\n"
        "s_cmp_eq_u32 s84, s86\n"
        "s_cselect_b32 s84, s85, s84\n"
        "v_mfma_f32_32x32x16_bf16 v[80:95], v[132:135], v[220:223], v[80:95]\n"
        "buffer_load_dwordx4 v[220:223], v21, s[40:43], s84 offen sc1\n"
        "ds_read_b64_tr_b16 v[124:125], v18 offset:32768\n"
        "ds_read_b64_tr_b16 v[126:127], v18 offset:33280\n"
        "s_waitcnt vmcnt(11) lgkmcnt(4)\n"
        "v_mfma_f32_32x32x16_bf16 v[64:79], v[112:115], v[224:227], v[64:79]\n"
        "ds_read_b64_tr_b16 v[128:129], v17 offset:34816\n"
        "ds_read_b64_tr_b16 v[130:131], v17 offset:35328\n"
        "s_add_u32 s84, s84, 1024\n"
        "s_cmp_eq_u32 s84, s86\n"
        "s_cselect_b32 s84, s85, s84\n"
        "v_mfma_f32_32x32x16_bf16 v[80:95], v[116:119], v[224:227], v[80:95]\n"
        "buffer_load_dwordx4 v[224:227], v21, s[40:43], s84 offen sc1\n"
        "ds_read_b64_tr_b16 v[132:133], v18 offset:34816\n"
        "ds_read_b64_tr_b16 v[134:135], v18 offset:35328\n"
        "s_waitcnt vmcnt(11) lgkmcnt(4)\n"
        "v_mfma_f32_32x32x16_bf16 v[64:79], v[120:123], v[228:231], v[64:79]\n"
        "ds_read_b64_tr_b16 v[112:113], v17 offset:36864\n"
        "ds_read_b64_tr_b16 v[114:115], v17 offset:37376\n"
        "s_add_u32 s84, s84, 1024\n"
        "s_cmp_eq_u32 s84, s86\n"
        "s_cselect_b32 s84, s85, s84\n"
        "v_mfma_f32_32x32x16_bf16 v[80:95], v[124:127], v[228:231], v[80:95]\n"
        "buffer_load_dwordx4 v[228:231], v21, s[40:43], s84 offen sc1\n"
        "ds_read_b64_tr_b16 v[116:117], v18 offset:36864\n"
        "ds_read_b64_tr_b16 v[118:119], v18 offset:37376\n"
        "s_waitcnt vmcnt(11) lgkmcnt(4)\n"
        "v_mfma_f32_32x32x16_bf16 v[64:79], v[128:131], v[232:235], v[64:79]\n"
        "ds_read_b64_tr_b16 v[120:121], v17 offset:38912\n"
        "ds_read_b64_tr_b16 v[122:123], v17 offset:39424\n"
        "s_add_u32 s84, s84, 1024\n"
        "s_cmp_eq_u32 s84, s86\n"
        "s_cselect_b32 s84, s85, s84\n"
        "v_mfma_f32_32x32x16_bf16 v[80:95], v[132:135], v[232:235], v[80:95]\n"
        "buffer_load_dwordx4 v[232:235], v21, s[40:43], s84 offen sc1\n"
        "ds_read_b64_tr_b16 v[124:125], v18 offset:38912\n"
        "ds_read_b64_tr_b16 v[126:127], v18 offset:39424\n"
        "s_waitcnt vmcnt(11) lgkmcnt(4)\n"
        "v_mfma_f32_32x32x16_bf16 v[64:79], v[112:115], v[236:239], v[64:79]\n"
        "ds_read_b64_tr_b16 v[128:129], v17 offset:40960\n"
        "ds_read_b64_tr_b16 v[130:131], v17 offset:41472\n"
        "s_add_u32 s84, s84, 1024\n"
        "s_cmp_eq_u32 s84, s86\n"
        "s_cselect_b32 s84, s85, s84\n"
        "v_mfma_f32_32x32x16_bf16 v[80:95], v[116:119], v[236:239], v[80:95]\n"
        "buffer_load_dwordx4 v[236:239], v21, s[40:43], s84 offen sc1\n"
        "ds_read_b64_tr_b16 v[132:133], v18 offset:40960\n"
        "ds_read_b64_tr_b16 v[134:135], v18 offset:41472\n"
        "s_waitcnt vmcnt(11) lgkmcnt(4)\n"
        "v_mfma_f32_32x32x16_bf16 v[64:79], v[120:123], v[240:243], v[64:79]\n"
        "ds_read_b64_tr_b16 v[112:113], v17 offset:43008\n"
        "ds_read_b64_tr_b16 v[114:115], v17 offset:43520\n"
        "s_add_u32 s84, s84, 1024\n"
        "s_cmp_eq_u32 s84, s86\n"
        "s_cselect_b32 s84, s85, s84\n"
        "v_mfma_f32_32x32x16_bf16 v[80:95], v[124:127], v[240:243], v[80:95]\n"
        "buffer_load_dwordx4 v[240:243], v21, s[40:43], s84 offen sc1\n"
        "ds_read_b64_tr_b16 v[116:117], v18 offset:43008\n"
        "ds_read_b64_tr_b16 v[118:119], v18 offset:43520\n"
        "s_waitcnt vmcnt(11) lgkmcnt(4)\n"
        "v_mfma_f32_32x32x16_bf16 v[64:79], v[128:131], v[244:247], v[64:79]\n"
        "ds_read_b64_tr_b16 v[120:121], v17 offset:45056\n"
        "ds_read_b64_tr_b16 v[122:123], v17 offset:45568\n"
        "s_add_u32 s84, s84, 1024\n"
        "s_cmp_eq_u32 s84, s86\n"
        "s_cselect_b32 s84, s85, s84\n"
        "v_mfma_f32_32x32x16_bf16 v[80:95], v[132:135], v[244:247], v[80:95]\n"
        "buffer_load_dwordx4 v[244:247], v21, s[40:43], s84 offen sc1\n"
        "ds_read_b64_tr_b16 v[124:125], v18 offset:45056\n"
        "ds_read_b64_tr_b16 v[126:127], v18 offset:45568\n"
        "s_waitcnt vmcnt(11) lgkmcnt(4)\n"
        "v_mfma_f32_32x32x16_bf16 v[64:79], v[112:115], v[200:203], v[64:79]\n"
        "ds_read_b64_tr_b16 v[128:129], v17 offset:47104\n"
        "ds_read_b64_tr_b16 v[130:131], v17 offset:47616\n"
        "s_add_u32 s84, s84, 1024\n"
        "s_cmp_eq_u32 s84, s86\n"
        "s_cselect_b32 s84, s85, s84\n"
        "v_mfma_f32_32x32x16_bf16 v[80:95], v[116:119], v[200:203], v[80:95]\n"
        "buffer_load_dwordx4 v[200:203], v21, s[40:43], s84 offen sc1\n"
        "ds_read_b64_tr_b16 v[132:133], v18 offset:47104\n"
        "ds_read_b64_tr_b16 v[134:135], v18 offset:47616\n"
        "s_waitcnt vmcnt(11) lgkmcnt(4)\n"
        "v_mfma_f32_32x32x16_bf16 v[64:79], v[120:123], v[204:207], v[64:79]\n"
        "ds_read_b64_tr_b16 v[112:113], v17 offset:49152\n"
        "ds_read_b64_tr_b16 v[114:115], v17 offset:49664\n"
        "s_add_u32 s84, s84, 1024\n"
        "s_cmp_eq_u32 s84, s86\n"
        "s_cselect_b32 s84, s85, s84\n"
        "v_mfma_f32_32x32x16_bf16 v[80:95], v[124:127], v[204:207], v[80:95]\n"
        "buffer_load_dwordx4 v[204:207], v21, s[40:43], s84 offen sc1\n"
        "ds_read_b64_tr_b16 v[116:117], v18 offset:49152\n"
        "ds_read_b64_tr_b16 v[118:119], v18 offset:49664\n"
        "s_waitcnt vmcnt(11) lgkmcnt(4)\n"
        "v_mfma_f32_32x32x16_bf16 v[64:79], v[128:131], v[208:211], v[64:79]\n"
        "ds_read_b64_tr_b16 v[120:121], v17 offset:51200\n"
        "ds_read_b64_tr_b16 v[122:123], v17 offset:51712\n"
        "s_add_u32 s84, s84, 1024\n"
        "s_cmp_eq_u32 s84, s86\n"
        "s_cselect_b32 s84, s85, s84\n"
        "v_mfma_f32_32x32x16_bf16 v[80:95], v[132:135], v[208:211], v[80:95]\n"
        "buffer_load_dwordx4 v[208:211], v21, s[40:43], s84 offen sc1\n"
        "ds_read_b64_tr_b16 v[124:125], v18 offset:51200\n"
        "ds_read_b64_tr_b16 v[126:127], v18 offset:51712\n"
        "s_waitcnt vmcnt(11) lgkmcnt(4)\n"
        "v_mfma_f32_32x32x16_bf16 v[64:79], v[112:115], v[212:215], v[64:79]\n"
        "ds_read_b64_tr_b16 v[128:129], v17 offset:53248\n"
        "ds_read_b64_tr_b16 v[130:131], v17 offset:53760\n"
        "s_add_u32 s84, s84, 1024\n"
        "s_cmp_eq_u32 s84, s86\n"
        "s_cselect_b32 s84, s85, s84\n"
        "v_mfma_f32_32x32x16_bf16 v[80:95], v[116:119], v[212:215], v[80:95]\n"
        "buffer_load_dwordx4 v[212:215], v21, s[40:43], s84 offen sc1\n"
        "ds_read_b64_tr_b16 v[132:133], v18 offset:53248\n"
        "ds_read_b64_tr_b16 v[134:135], v18 offset:53760\n"
        "s_waitcnt vmcnt(11) lgkmcnt(4)\n"
        "v_mfma_f32_32x32x16_bf16 v[64:79], v[120:123], v[216:219], v[64:79]\n"
        "ds_read_b64_tr_b16 v[112:113], v17 offset:55296\n"
        "ds_read_b64_tr_b16 v[114:115], v17 offset:55808\n"
        "s_add_u32 s84, s84, 1024\n"
        "s_cmp_eq_u32 s84, s86\n"
        "s_cselect_b32 s84, s85, s84\n"
        "v_mfma_f32_32x32x16_bf16 v[80:95], v[124:127], v[216:219], v[80:95]\n"
        "buffer_load_dwordx4 v[216:219], v21, s[40:43], s84 offen sc1\n"
        "ds_read_b64_tr_b16 v[116:117], v18 offset:55296\n"
        "ds_read_b64_tr_b16 v[118:119], v18 offset:55808\n"
        "s_waitcnt vmcnt(11) lgkmcnt(4)\n"
        "v_mfma_f32_32x32x16_bf16 v[64:79], v[128:131], v[220:223], v[64:79]\n"
        "ds_read_b64_tr_b16 v[120:121], v17 offset:57344\n"
        "ds_read_b64_tr_b16 v[122:123], v17 offset:57856\n"
        "s_add_u32 s84, s84, 1024\n"
        "s_cmp_eq_u32 s84, s86\n"
        "s_cselect_b32 s84, s85, s84\n"
        "v_mfma_f32_32x32x16_bf16 v[80:95], v[132:135], v[220:223], v[80:95]\n"
        "buffer_load_dwordx4 v[220:223], v21, s[40:43], s84 offen sc1\n"
        "ds_read_b64_tr_b16 v[124:125], v18 offset:57344\n"
        "ds_read_b64_tr_b16 v[126:127], v18 offset:57856\n"
        "s_waitcnt vmcnt(11) lgkmcnt(4)\n"
        "v_mfma_f32_32x32x16_bf16 v[64:79], v[112:115], v[224:227], v[64:79]\n"
        "ds_read_b64_tr_b16 v[128:129], v17 offset:59392\n"
        "ds_read_b64_tr_b16 v[130:131], v17 offset:59904\n"
        "s_add_u32 s84, s84, 1024\n"
        "s_cmp_eq_u32 s84, s86\n"
        "s_cselect_b32 s84, s85, s84\n"
        "v_mfma_f32_32x32x16_bf16 v[80:95], v[116:119], v[224:227], v[80:95]\n"
        "buffer_load_dwordx4 v[224:227], v21, s[40:43], s84 offen sc1\n"
        "ds_read_b64_tr_b16 v[132:133], v18 offset:59392\n"
        "ds_read_b64_tr_b16 v[134:135], v18 offset:59904\n"
        "s_waitcnt vmcnt(11) lgkmcnt(4)\n"
        "v_mfma_f32_32x32x16_bf16 v[64:79], v[120:123], v[228:231], v[64:79]\n"
        "ds_read_b64_tr_b16 v[112:113], v17 offset:61440\n"
        "ds_read_b64_tr_b16 v[114:115], v17 offset:61952\n"
        "s_add_u32 s84, s84, 1024\n"
        "s_cmp_eq_u32 s84, s86\n"
        "s_cselect_b32 s84, s85, s84\n"
        "v_mfma_f32_32x32x16_bf16 v[80:95], v[124:127], v[228:231], v[80:95]\n"
        "ds_read_b64_tr_b16 v[116:117], v18 offset:61440\n"
        "ds_read_b64_tr_b16 v[118:119], v18 offset:61952\n"
        "s_waitcnt vmcnt(10) lgkmcnt(4)\n"
        "v_mfma_f32_32x32x16_bf16 v[64:79], v[128:131], v[232:235], v[64:79]\n"
        "ds_read_b64_tr_b16 v[120:121], v17 offset:63488\n"
        "ds_read_b64_tr_b16 v[122:123], v17 offset:64000\n"
        "v_mfma_f32_32x32x16_bf16 v[80:95], v[132:135], v[232:235], v[80:95]\n"
        "ds_read_b64_tr_b16 v[124:125], v18 offset:63488\n"
        "ds_read_b64_tr_b16 v[126:127], v18 offset:64000\n"
        "s_waitcnt vmcnt(9) lgkmcnt(4)\n"
        "v_mfma_f32_32x32x16_bf16 v[64:79], v[112:115], v[236:239], v[64:79]\n"
        "ds_read_b64_tr_b16 v[128:129], v19 offset:0\n"
        "ds_read_b64_tr_b16 v[130:131], v19 offset:512\n"
        "v_mfma_f32_32x32x16_bf16 v[80:95], v[116:119], v[236:239], v[80:95]\n"
        "ds_read_b64_tr_b16 v[132:133], v20 offset:0\n"
        "ds_read_b64_tr_b16 v[134:135], v20 offset:512\n"
        "s_waitcnt vmcnt(8) lgkmcnt(4)\n"
        "v_mfma_f32_32x32x16_bf16 v[64:79], v[120:123], v[240:243], v[64:79]\n"
        "ds_read_b64_tr_b16 v[112:113], v19 offset:2048\n"
        "ds_read_b64_tr_b16 v[114:115], v19 offset:2560\n"
        "v_mfma_f32_32x32x16_bf16 v[80:95], v[124:127], v[240:243], v[80:95]\n"
        "ds_read_b64_tr_b16 v[116:117], v20 offset:2048\n"
        "ds_read_b64_tr_b16 v[118:119], v20 offset:2560\n"
        "s_waitcnt vmcnt(7) lgkmcnt(4)\n"
        "v_mfma_f32_32x32x16_bf16 v[64:79], v[128:131], v[244:247], v[64:79]\n"
        "ds_read_b64_tr_b16 v[120:121], v19 offset:4096\n"
        "ds_read_b64_tr_b16 v[122:123], v19 offset:4608\n"
        "v_mfma_f32_32x32x16_bf16 v[80:95], v[132:135], v[244:247], v[80:95]\n"
        "ds_read_b64_tr_b16 v[124:125], v20 offset:4096\n"
        "ds_read_b64_tr_b16 v[126:127], v20 offset:4608\n"
        "s_waitcnt vmcnt(6) lgkmcnt(4)\n"
        "v_mfma_f32_32x32x16_bf16 v[64:79], v[112:115], v[200:203], v[64:79]\n"
        "ds_read_b64_tr_b16 v[128:129], v19 offset:6144\n"
        "ds_read_b64_tr_b16 v[130:131], v19 offset:6656\n"
        "v_mfma_f32_32x32x16_bf16 v[80:95], v[116:119], v[200:203], v[80:95]\n"
        "ds_read_b64_tr_b16 v[132:133], v20 offset:6144\n"
        "ds_read_b64_tr_b16 v[134:135], v20 offset:6656\n"
        "s_waitcnt vmcnt(5) lgkmcnt(4)\n"
        "v_mfma_f32_32x32x16_bf16 v[64:79], v[120:123], v[204:207], v[64:79]\n"
        "ds_read_b64_tr_b16 v[112:113], v19 offset:8192\n"
        "ds_read_b64_tr_b16 v[114:115], v19 offset:8704\n"
        "v_mfma_f32_32x32x16_bf16 v[80:95], v[124:127], v[204:207], v[80:95]\n"
        "ds_read_b64_tr_b16 v[116:117], v20 offset:8192\n"
        "ds_read_b64_tr_b16 v[118:119], v20 offset:8704\n"
        "s_waitcnt vmcnt(4) lgkmcnt(4)\n"
        "v_mfma_f32_32x32x16_bf16 v[64:79], v[128:131], v[208:211], v[64:79]\n"
        "ds_read_b64_tr_b16 v[120:121], v19 offset:10240\n"
        "ds_read_b64_tr_b16 v[122:123], v19 offset:10752\n"
        "v_mfma_f32_32x32x16_bf16 v[80:95], v[132:135], v[208:211], v[80:95]\n"
        "ds_read_b64_tr_b16 v[124:125], v20 offset:10240\n"
        "ds_read_b64_tr_b16 v[126:127], v20 offset:10752\n"
        "s_waitcnt vmcnt(3) lgkmcnt(4)\n"
        "v_mfma_f32_32x32x16_bf16 v[64:79], v[112:115], v[212:215], v[64:79]\n"
        "ds_read_b64_tr_b16 v[128:129], v19 offset:12288\n"
        "ds_read_b64_tr_b16 v[130:131], v19 offset:12800\n"
        "v_mfma_f32_32x32x16_bf16 v[80:95], v[116:119], v[212:215], v[80:95]\n"
        "ds_read_b64_tr_b16 v[132:133], v20 offset:12288\n"
        "ds_read_b64_tr_b16 v[134:135], v20 offset:12800\n"
        "s_waitcnt vmcnt(2) lgkmcnt(4)\n"
        "v_mfma_f32_32x32x16_bf16 v[64:79], v[120:123], v[216:219], v[64:79]\n"
        "ds_read_b64_tr_b16 v[112:113], v19 offset:14336\n"
        "ds_read_b64_tr_b16 v[114:115], v19 offset:14848\n"
        "v_mfma_f32_32x32x16_bf16 v[80:95], v[124:127], v[216:219], v[80:95]\n"
        "ds_read_b64_tr_b16 v[116:117], v20 offset:14336\n"
        "ds_read_b64_tr_b16 v[118:119], v20 offset:14848\n"
        "s_waitcnt vmcnt(1) lgkmcnt(4)\n"
        "v_mfma_f32_32x32x16_bf16 v[64:79], v[128:131], v[220:223], v[64:79]\n"
        "v_mfma_f32_32x32x16_bf16 v[80:95], v[132:135], v[220:223], v[80:95]\n"
        "s_waitcnt vmcnt(0) lgkmcnt(0)\n"
        "v_mfma_f32_32x32x16_bf16 v[64:79], v[112:115], v[224:227], v[64:79]\n"
        "v_mfma_f32_32x32x16_bf16 v[80:95], v[116:119], v[224:227], v[80:95]\n"
        "s_lshl_b32 s64, s48, 12\n"
        "v_add_u32 v2, s64, v21\n"
        "v_xor_b32 v3, 4096, v2\n"
        "v_and_b32 v4, 31, v1\n"
        "v_lshrrev_b32 v5, 5, v1\n"
        "v_lshlrev_b32 v4, 10, v4\n"
        "v_lshl_add_u32 v4, v5, 4, v4\n"
        "s_lshl_b32 s64, s89, 15\n"
        "s_lshl_b32 s65, s56, 7\n"
        "s_add_u32 s64, s64, s65\n"
        "s_lshl_b32 s65, s87, 6\n"
        "s_add_u32 s64, s64, s65\n"
        "s_barrier\n"
        "s_nop 1\n"
        "ds_write_b128 v2, v[64:67] offset:0\n"
        "ds_write_b128 v2, v[68:71] offset:1024\n"
        "ds_write_b128 v2, v[72:75] offset:2048\n"
        "ds_write_b128 v2, v[76:79] offset:3072\n"
        "s_waitcnt lgkmcnt(0)\n"
        "s_barrier\n"
        "ds_read_b128 v[112:115], v3 offset:0\n"
        "ds_read_b128 v[116:119], v3 offset:1024\n"
        "ds_read_b128 v[120:123], v3 offset:2048\n"
        "ds_read_b128 v[124:127], v3 offset:3072\n"
        "s_waitcnt lgkmcnt(3)\n"
        "v_pk_add_f32 v[80:81], v[80:81], v[112:113]\n"
        "v_pk_add_f32 v[82:83], v[82:83], v[114:115]\n"
        "s_waitcnt lgkmcnt(2)\n"
        "v_pk_add_f32 v[84:85], v[84:85], v[116:117]\n"
        "v_pk_add_f32 v[86:87], v[86:87], v[118:119]\n"
        "s_nop 0\n"
        "v_cvt_pk_bf16_f32 v128, v80, v81\n"
        "v_cvt_pk_bf16_f32 v129, v82, v83\n"
        "v_cvt_pk_bf16_f32 v130, v84, v85\n"
        "v_cvt_pk_bf16_f32 v131, v86, v87\n"
        "buffer_store_dwordx4 v[128:131], v4, s[44:47], s64 offen offset:0\n"
        "s_waitcnt lgkmcnt(1)\n"
        "v_pk_add_f32 v[88:89], v[88:89], v[120:121]\n"
        "v_pk_add_f32 v[90:91], v[90:91], v[122:123]\n"
        "s_waitcnt lgkmcnt(0)\n"
        "v_pk_add_f32 v[92:93], v[92:93], v[124:125]\n"
        "v_pk_add_f32 v[94:95], v[94:95], v[126:127]\n"
        "s_nop 0\n"
        "v_cvt_pk_bf16_f32 v132, v88, v89\n"
        "v_cvt_pk_bf16_f32 v133, v90, v91\n"
        "v_cvt_pk_bf16_f32 v134, v92, v93\n"
        "v_cvt_pk_bf16_f32 v135, v94, v95\n"
        "buffer_store_dwordx4 v[132:135], v4, s[44:47], s64 offen offset:32\n"
        "s_endpgm\n"
        "Lloader_%=:\n"
        "s_and_b32 s49, s48, 1\n"
        "v_lshrrev_b32 v2, 3, v1\n"
        "v_and_b32 v3, 7, v1\n"
        "v_lshrrev_b32 v5, 4, v1\n"
        "v_xor_b32 v6, v3, v5\n"
        "v_lshlrev_b32 v6, 4, v6\n"
        "v_lshl_add_u32 v10, v2, 10, v6\n"
        "v_xor_b32 v11, 64, v10\n"
        "v_add_u32 v12, 14336, v10\n"
        "v_add_u32 v11, 7168, v11\n"
        "v_add_u32 v13, 14336, v11\n"
        "s_movk_i32 s65, 2752\n"
        "v_add_u32 v7, 0, v1\n"
        "v_mul_u32_u24 v8, 0xaaab, v7\n"
        "v_lshrrev_b32 v8, 19, v8\n"
        "v_mul_u32_u24 v9, 12, v8\n"
        "v_sub_u32 v9, v7, v9\n"
        "v_mul_u32_u24 v8, s65, v8\n"
        "v_lshl_add_u32 v14, v9, 4, v8\n"
        "v_add_u32 v7, 64, v1\n"
        "v_mul_u32_u24 v8, 0xaaab, v7\n"
        "v_lshrrev_b32 v8, 19, v8\n"
        "v_mul_u32_u24 v9, 12, v8\n"
        "v_sub_u32 v9, v7, v9\n"
        "v_mul_u32_u24 v8, s65, v8\n"
        "v_lshl_add_u32 v15, v9, 4, v8\n"
        "v_add_u32 v7, 128, v1\n"
        "v_mul_u32_u24 v8, 0xaaab, v7\n"
        "v_lshrrev_b32 v8, 19, v8\n"
        "v_mul_u32_u24 v9, 12, v8\n"
        "v_sub_u32 v9, v7, v9\n"
        "v_mul_u32_u24 v8, s65, v8\n"
        "v_lshl_add_u32 v16, v9, 4, v8\n"
        "v_add_u32 v17, 40960, v14\n"
        "v_add_u32 v18, 44032, v15\n"
        "v_add_u32 v19, 43008, v16\n"
        "v_subrev_u32 v15, 1024, v15\n"
        "v_subrev_u32 v16, 2048, v16\n"
        "s_lshl_b32 s66, s58, 1\n"
        "s_add_u32 s95, s56, s66\n"
        "s_lshl_b32 s74, s50, 17\n"
        "s_lshl_b32 s64, s49, 16\n"
        "s_add_u32 s74, s74, s64\n"
        "s_lshl_b32 s75, s52, 6\n"
        "s_mul_i32 s64, s49, 88064\n"
        "s_add_u32 s75, s75, s64\n"
        "s_lshl_b32 s76, s49, 13\n"
        "s_mul_i32 s77, s49, 6144\n"
        "s_add_u32 s77, s77, 16384\n"
        "s_mov_b32 s94, 0\n"
        "s_mov_b32 s22, 2097152\n"
        "s_mov_b32 s23, 0x20000\n"
        "s_mov_b32 s26, 1409024\n"
        "s_mov_b32 s27, 0x20000\n"
        "s_waitcnt lgkmcnt(0)\n"
        "s_mov_b32 s20, s4\n"
        "s_and_b32 s21, s5, 0xffff\n"
        "s_mov_b32 s24, s6\n"
        "s_and_b32 s25, s7, 0xffff\n"
        "s_and_b32 s64, s95, 7\n"
        "s_mul_i32 s65, s64, 176128\n"
        "s_lshl_b32 s64, s64, 7\n"
        "s_add_u32 s64, s64, s74\n"
        "s_add_u32 s65, s65, s75\n"
        "s_add_u32 m0, s94, s77\n"
        "s_add_u32 s95, s95, 1\n"
        "buffer_load_dwordx4 v14, s[24:27], s65 offen offset:0 lds\n"
        "buffer_load_dwordx4 v15, s[24:27], s65 offen offset:1024 lds\n"
        "buffer_load_dwordx4 v16, s[24:27], s65 offen offset:2048 lds\n"
        "buffer_load_dwordx4 v17, s[24:27], s65 offen offset:3072 lds\n"
        "s_add_u32 m0, m0, 4096\n"
        "s_nop 0\n"
        "buffer_load_dwordx4 v18, s[24:27], s65 offen offset:0 lds\n"
        "buffer_load_dwordx4 v19, s[24:27], s65 offen offset:1024 lds\n"
        "s_add_u32 m0, s94, s76\n"
        "s_nop 0\n"
        "buffer_load_dwordx4 v10, s[20:23], s64 offen offset:0 lds\n"
        "buffer_load_dwordx4 v11, s[20:23], s64 offen offset:1024 lds\n"
        "buffer_load_dwordx4 v12, s[20:23], s64 offen offset:2048 lds\n"
        "buffer_load_dwordx4 v13, s[20:23], s64 offen offset:3072 lds\n"
        "s_add_u32 m0, m0, 4096\n"
        "s_add_u32 s64, s64, 32768\n"
        "buffer_load_dwordx4 v10, s[20:23], s64 offen offset:0 lds\n"
        "buffer_load_dwordx4 v11, s[20:23], s64 offen offset:1024 lds\n"
        "buffer_load_dwordx4 v12, s[20:23], s64 offen offset:2048 lds\n"
        "buffer_load_dwordx4 v13, s[20:23], s64 offen offset:3072 lds\n"
        "s_add_u32 s94, s94, 28672\n"
        "s_cmp_eq_u32 s94, 143360\n"
        "s_cselect_b32 s94, 0, s94\n"
        "s_and_b32 s64, s95, 7\n"
        "s_mul_i32 s65, s64, 176128\n"
        "s_lshl_b32 s64, s64, 7\n"
        "s_add_u32 s64, s64, s74\n"
        "s_add_u32 s65, s65, s75\n"
        "s_add_u32 m0, s94, s77\n"
        "s_add_u32 s95, s95, 1\n"
        "buffer_load_dwordx4 v14, s[24:27], s65 offen offset:0 lds\n"
        "buffer_load_dwordx4 v15, s[24:27], s65 offen offset:1024 lds\n"
        "buffer_load_dwordx4 v16, s[24:27], s65 offen offset:2048 lds\n"
        "buffer_load_dwordx4 v17, s[24:27], s65 offen offset:3072 lds\n"
        "s_add_u32 m0, m0, 4096\n"
        "s_nop 0\n"
        "buffer_load_dwordx4 v18, s[24:27], s65 offen offset:0 lds\n"
        "buffer_load_dwordx4 v19, s[24:27], s65 offen offset:1024 lds\n"
        "s_add_u32 m0, s94, s76\n"
        "s_nop 0\n"
        "buffer_load_dwordx4 v10, s[20:23], s64 offen offset:0 lds\n"
        "buffer_load_dwordx4 v11, s[20:23], s64 offen offset:1024 lds\n"
        "buffer_load_dwordx4 v12, s[20:23], s64 offen offset:2048 lds\n"
        "buffer_load_dwordx4 v13, s[20:23], s64 offen offset:3072 lds\n"
        "s_add_u32 m0, m0, 4096\n"
        "s_add_u32 s64, s64, 32768\n"
        "buffer_load_dwordx4 v10, s[20:23], s64 offen offset:0 lds\n"
        "buffer_load_dwordx4 v11, s[20:23], s64 offen offset:1024 lds\n"
        "buffer_load_dwordx4 v12, s[20:23], s64 offen offset:2048 lds\n"
        "buffer_load_dwordx4 v13, s[20:23], s64 offen offset:3072 lds\n"
        "s_add_u32 s94, s94, 28672\n"
        "s_cmp_eq_u32 s94, 143360\n"
        "s_cselect_b32 s94, 0, s94\n"
        "s_and_b32 s64, s95, 7\n"
        "s_mul_i32 s65, s64, 176128\n"
        "s_lshl_b32 s64, s64, 7\n"
        "s_add_u32 s64, s64, s74\n"
        "s_add_u32 s65, s65, s75\n"
        "s_add_u32 m0, s94, s77\n"
        "s_add_u32 s95, s95, 1\n"
        "buffer_load_dwordx4 v14, s[24:27], s65 offen offset:0 lds\n"
        "buffer_load_dwordx4 v15, s[24:27], s65 offen offset:1024 lds\n"
        "buffer_load_dwordx4 v16, s[24:27], s65 offen offset:2048 lds\n"
        "buffer_load_dwordx4 v17, s[24:27], s65 offen offset:3072 lds\n"
        "s_add_u32 m0, m0, 4096\n"
        "s_nop 0\n"
        "buffer_load_dwordx4 v18, s[24:27], s65 offen offset:0 lds\n"
        "buffer_load_dwordx4 v19, s[24:27], s65 offen offset:1024 lds\n"
        "s_add_u32 m0, s94, s76\n"
        "s_nop 0\n"
        "buffer_load_dwordx4 v10, s[20:23], s64 offen offset:0 lds\n"
        "buffer_load_dwordx4 v11, s[20:23], s64 offen offset:1024 lds\n"
        "buffer_load_dwordx4 v12, s[20:23], s64 offen offset:2048 lds\n"
        "buffer_load_dwordx4 v13, s[20:23], s64 offen offset:3072 lds\n"
        "s_add_u32 m0, m0, 4096\n"
        "s_add_u32 s64, s64, 32768\n"
        "buffer_load_dwordx4 v10, s[20:23], s64 offen offset:0 lds\n"
        "buffer_load_dwordx4 v11, s[20:23], s64 offen offset:1024 lds\n"
        "buffer_load_dwordx4 v12, s[20:23], s64 offen offset:2048 lds\n"
        "buffer_load_dwordx4 v13, s[20:23], s64 offen offset:3072 lds\n"
        "s_add_u32 s94, s94, 28672\n"
        "s_cmp_eq_u32 s94, 143360\n"
        "s_cselect_b32 s94, 0, s94\n"
        "v_lshrrev_b32 v2, 3, v1\n"
        "v_and_b32 v3, 7, v1\n"
        "v_bfe_u32 v5, v1, 4, 1\n"
        "v_lshlrev_b32 v5, 2, v5\n"
        "v_xor_b32 v6, v3, v5\n"
        "v_lshlrev_b32 v6, 4, v6\n"
        "v_lshl_add_u32 v20, v2, 10, v6\n"
        "s_mul_i32 s66, s56, 5\n"
        "s_mul_i32 s67, s58, 11\n"
        "s_add_u32 s57, s66, s67\n"
        "s_sub_u32 s66, s57, 43\n"
        "s_cselect_b32 s57, s57, s66\n"
        "s_lshl_b32 s79, s56, 7\n"
        "s_lshl_b32 s78, s57, 4\n"
        "s_mov_b32 s73, 0\n"
        "s_sub_u32 s28, s10, 4096\n"
        "s_subb_u32 s29, s11, 0\n"
        "s_and_b32 s29, s29, 0xffff\n"
        "s_mov_b32 s30, 1413120\n"
        "s_mov_b32 s31, 0x20000\n"
        "s_mov_b32 s40, s16\n"
        "s_and_b32 s41, s17, 0xffff\n"
        "s_mov_b32 s42, 6692864\n"
        "s_mov_b32 s43, 0x20000\n"
        "Llloop_%=:\n"
        "s_waitcnt vmcnt(28)\n"
        "s_barrier\n"
        "s_and_b32 s64, s95, 7\n"
        "s_mul_i32 s65, s64, 176128\n"
        "s_lshl_b32 s64, s64, 7\n"
        "s_add_u32 s64, s64, s74\n"
        "s_add_u32 s65, s65, s75\n"
        "s_add_u32 m0, s94, s77\n"
        "s_add_u32 s95, s95, 1\n"
        "buffer_load_dwordx4 v14, s[24:27], s65 offen offset:0 lds\n"
        "buffer_load_dwordx4 v15, s[24:27], s65 offen offset:1024 lds\n"
        "buffer_load_dwordx4 v16, s[24:27], s65 offen offset:2048 lds\n"
        "buffer_load_dwordx4 v17, s[24:27], s65 offen offset:3072 lds\n"
        "s_add_u32 m0, m0, 4096\n"
        "s_nop 0\n"
        "buffer_load_dwordx4 v18, s[24:27], s65 offen offset:0 lds\n"
        "buffer_load_dwordx4 v19, s[24:27], s65 offen offset:1024 lds\n"
        "s_add_u32 m0, s94, s76\n"
        "s_nop 0\n"
        "buffer_load_dwordx4 v10, s[20:23], s64 offen offset:0 lds\n"
        "buffer_load_dwordx4 v11, s[20:23], s64 offen offset:1024 lds\n"
        "buffer_load_dwordx4 v12, s[20:23], s64 offen offset:2048 lds\n"
        "buffer_load_dwordx4 v13, s[20:23], s64 offen offset:3072 lds\n"
        "s_add_u32 m0, m0, 4096\n"
        "s_add_u32 s64, s64, 32768\n"
        "buffer_load_dwordx4 v10, s[20:23], s64 offen offset:0 lds\n"
        "buffer_load_dwordx4 v11, s[20:23], s64 offen offset:1024 lds\n"
        "buffer_load_dwordx4 v12, s[20:23], s64 offen offset:2048 lds\n"
        "buffer_load_dwordx4 v13, s[20:23], s64 offen offset:3072 lds\n"
        "s_add_u32 s94, s94, 28672\n"
        "s_cmp_eq_u32 s94, 143360\n"
        "s_cselect_b32 s94, 0, s94\n"
        "s_add_u32 s73, s73, 1\n"
        "s_cmp_lt_u32 s73, 5\n"
        "s_cbranch_scc1 Llloop_%=\n"
        "s_lshl_b32 s67, s49, 2\n"
        "s_add_u32 s67, s67, 84\n"
        "s_lshl_b32 s64, s67, 3\n"
        "s_add_u32 s64, s64, s78\n"
        "s_sub_u32 s64, s64, 592\n"
        "s_sub_u32 s66, s64, 688\n"
        "s_cselect_b32 s64, s64, s66\n"
        "s_lshl_b32 s96, s64, 10\n"
        "s_add_u32 s98, s79, 704512\n"
        "s_add_u32 s96, s96, s98\n"
        "s_add_u32 s97, s98, 704512\n"
        "s_lshl_b32 s99, s67, 10\n"
        "s_waitcnt vmcnt(28)\n"
        "s_barrier\n"
        "s_add_u32 s68, s96, 4096\n"
        "s_add_u32 s96, s96, 8192\n"
        "s_cmp_eq_u32 s96, s97\n"
        "s_cselect_b32 s96, s98, s96\n"
        "s_add_u32 s69, s96, 3072\n"
        "s_add_u32 s96, s96, 8192\n"
        "s_cmp_eq_u32 s96, s97\n"
        "s_cselect_b32 s96, s98, s96\n"
        "s_add_u32 s70, s96, 2048\n"
        "s_add_u32 s96, s96, 8192\n"
        "s_cmp_eq_u32 s96, s97\n"
        "s_cselect_b32 s96, s98, s96\n"
        "s_add_u32 s71, s96, 1024\n"
        "s_add_u32 s96, s96, 8192\n"
        "s_cmp_eq_u32 s96, s97\n"
        "s_cselect_b32 s96, s98, s96\n"
        "s_mov_b32 m0, s99\n"
        "s_add_u32 s96, s96, 32768\n"
        "s_sub_u32 s66, s96, 704512\n"
        "s_cmp_ge_u32 s96, s97\n"
        "s_cselect_b32 s96, s66, s96\n"
        "s_add_u32 s99, s99, 8192\n"
        "buffer_load_dwordx4 v20, s[28:31], s68 offen offset:0 lds\n"
        "buffer_load_dwordx4 v20, s[28:31], s69 offen offset:1024 lds\n"
        "buffer_load_dwordx4 v20, s[28:31], s70 offen offset:2048 lds\n"
        "buffer_load_dwordx4 v20, s[28:31], s71 offen offset:3072 lds\n"
        "s_add_u32 s68, s96, 4096\n"
        "s_add_u32 s96, s96, 8192\n"
        "s_cmp_eq_u32 s96, s97\n"
        "s_cselect_b32 s96, s98, s96\n"
        "s_add_u32 s69, s96, 3072\n"
        "s_add_u32 s96, s96, 8192\n"
        "s_cmp_eq_u32 s96, s97\n"
        "s_cselect_b32 s96, s98, s96\n"
        "s_add_u32 s70, s96, 2048\n"
        "s_add_u32 s96, s96, 8192\n"
        "s_cmp_eq_u32 s96, s97\n"
        "s_cselect_b32 s96, s98, s96\n"
        "s_add_u32 s71, s96, 1024\n"
        "s_add_u32 s96, s96, 8192\n"
        "s_cmp_eq_u32 s96, s97\n"
        "s_cselect_b32 s96, s98, s96\n"
        "s_mov_b32 m0, s99\n"
        "s_add_u32 s96, s96, 32768\n"
        "s_sub_u32 s66, s96, 704512\n"
        "s_cmp_ge_u32 s96, s97\n"
        "s_cselect_b32 s96, s66, s96\n"
        "s_add_u32 s99, s99, 8192\n"
        "buffer_load_dwordx4 v20, s[28:31], s68 offen offset:0 lds\n"
        "buffer_load_dwordx4 v20, s[28:31], s69 offen offset:1024 lds\n"
        "buffer_load_dwordx4 v20, s[28:31], s70 offen offset:2048 lds\n"
        "buffer_load_dwordx4 v20, s[28:31], s71 offen offset:3072 lds\n"
        "s_mov_b32 s74, s96\n"
        "s_mov_b32 s75, s99\n"
        "s_lshl_b32 s96, s78, 10\n"
        "s_mul_i32 s98, s49, 704512\n"
        "s_add_u32 s98, s98, s79\n"
        "s_add_u32 s96, s96, s98\n"
        "s_add_u32 s97, s98, 704512\n"
        "s_mul_i32 s99, s49, 6144\n"
        "s_add_u32 s99, s99, 151552\n"
        "s_waitcnt vmcnt(22)\n"
        "s_barrier\n"
        "s_add_u32 s68, s96, 4096\n"
        "s_add_u32 s96, s96, 8192\n"
        "s_cmp_eq_u32 s96, s97\n"
        "s_cselect_b32 s96, s98, s96\n"
        "s_add_u32 s69, s96, 3072\n"
        "s_add_u32 s96, s96, 8192\n"
        "s_cmp_eq_u32 s96, s97\n"
        "s_cselect_b32 s96, s98, s96\n"
        "s_add_u32 s70, s96, 2048\n"
        "s_add_u32 s96, s96, 8192\n"
        "s_cmp_eq_u32 s96, s97\n"
        "s_cselect_b32 s96, s98, s96\n"
        "s_add_u32 s71, s96, 1024\n"
        "s_add_u32 s96, s96, 8192\n"
        "s_cmp_eq_u32 s96, s97\n"
        "s_cselect_b32 s96, s98, s96\n"
        "s_mov_b32 m0, s99\n"
        "s_nop 0\n"
        "s_add_u32 s99, s99, 4096\n"
        "buffer_load_dwordx4 v20, s[28:31], s68 offen offset:0 lds\n"
        "buffer_load_dwordx4 v20, s[28:31], s69 offen offset:1024 lds\n"
        "buffer_load_dwordx4 v20, s[28:31], s70 offen offset:2048 lds\n"
        "buffer_load_dwordx4 v20, s[28:31], s71 offen offset:3072 lds\n"
        "s_add_u32 s68, s96, 4096\n"
        "s_add_u32 s96, s96, 8192\n"
        "s_cmp_eq_u32 s96, s97\n"
        "s_cselect_b32 s96, s98, s96\n"
        "s_add_u32 s69, s96, 3072\n"
        "s_add_u32 s96, s96, 8192\n"
        "s_cmp_eq_u32 s96, s97\n"
        "s_cselect_b32 s96, s98, s96\n"
        "s_mov_b32 m0, s99\n"
        "s_nop 0\n"
        "s_add_u32 s99, s99, 2048\n"
        "buffer_load_dwordx4 v20, s[28:31], s68 offen offset:0 lds\n"
        "buffer_load_dwordx4 v20, s[28:31], s69 offen offset:1024 lds\n"
        "s_mov_b32 s96, s74\n"
        "s_mov_b32 s99, s75\n"
        "s_add_u32 s98, s79, 704512\n"
        "s_add_u32 s97, s98, 704512\n"
        "s_add_u32 s68, s96, 4096\n"
        "s_add_u32 s96, s96, 8192\n"
        "s_cmp_eq_u32 s96, s97\n"
        "s_cselect_b32 s96, s98, s96\n"
        "s_add_u32 s69, s96, 3072\n"
        "s_add_u32 s96, s96, 8192\n"
        "s_cmp_eq_u32 s96, s97\n"
        "s_cselect_b32 s96, s98, s96\n"
        "s_add_u32 s70, s96, 2048\n"
        "s_add_u32 s96, s96, 8192\n"
        "s_cmp_eq_u32 s96, s97\n"
        "s_cselect_b32 s96, s98, s96\n"
        "s_add_u32 s71, s96, 1024\n"
        "s_add_u32 s96, s96, 8192\n"
        "s_cmp_eq_u32 s96, s97\n"
        "s_cselect_b32 s96, s98, s96\n"
        "s_mov_b32 m0, s99\n"
        "s_add_u32 s96, s96, 32768\n"
        "s_sub_u32 s66, s96, 704512\n"
        "s_cmp_ge_u32 s96, s97\n"
        "s_cselect_b32 s96, s66, s96\n"
        "s_add_u32 s99, s99, 8192\n"
        "buffer_load_dwordx4 v20, s[28:31], s68 offen offset:0 lds\n"
        "buffer_load_dwordx4 v20, s[28:31], s69 offen offset:1024 lds\n"
        "buffer_load_dwordx4 v20, s[28:31], s70 offen offset:2048 lds\n"
        "buffer_load_dwordx4 v20, s[28:31], s71 offen offset:3072 lds\n"
        "s_add_u32 s68, s96, 4096\n"
        "s_add_u32 s96, s96, 8192\n"
        "s_cmp_eq_u32 s96, s97\n"
        "s_cselect_b32 s96, s98, s96\n"
        "s_add_u32 s69, s96, 3072\n"
        "s_add_u32 s96, s96, 8192\n"
        "s_cmp_eq_u32 s96, s97\n"
        "s_cselect_b32 s96, s98, s96\n"
        "s_add_u32 s70, s96, 2048\n"
        "s_add_u32 s96, s96, 8192\n"
        "s_cmp_eq_u32 s96, s97\n"
        "s_cselect_b32 s96, s98, s96\n"
        "s_add_u32 s71, s96, 1024\n"
        "s_add_u32 s96, s96, 8192\n"
        "s_cmp_eq_u32 s96, s97\n"
        "s_cselect_b32 s96, s98, s96\n"
        "s_mov_b32 m0, s99\n"
        "s_add_u32 s96, s96, 32768\n"
        "s_sub_u32 s66, s96, 704512\n"
        "s_cmp_ge_u32 s96, s97\n"
        "s_cselect_b32 s96, s66, s96\n"
        "s_add_u32 s99, s99, 8192\n"
        "buffer_load_dwordx4 v20, s[28:31], s68 offen offset:0 lds\n"
        "buffer_load_dwordx4 v20, s[28:31], s69 offen offset:1024 lds\n"
        "buffer_load_dwordx4 v20, s[28:31], s70 offen offset:2048 lds\n"
        "buffer_load_dwordx4 v20, s[28:31], s71 offen offset:3072 lds\n"
        "s_waitcnt vmcnt(22)\n"
        "s_barrier\n"
        "s_add_u32 s68, s96, 4096\n"
        "s_add_u32 s96, s96, 8192\n"
        "s_cmp_eq_u32 s96, s97\n"
        "s_cselect_b32 s96, s98, s96\n"
        "s_add_u32 s69, s96, 3072\n"
        "s_add_u32 s96, s96, 8192\n"
        "s_cmp_eq_u32 s96, s97\n"
        "s_cselect_b32 s96, s98, s96\n"
        "s_add_u32 s70, s96, 2048\n"
        "s_add_u32 s96, s96, 8192\n"
        "s_cmp_eq_u32 s96, s97\n"
        "s_cselect_b32 s96, s98, s96\n"
        "s_add_u32 s71, s96, 1024\n"
        "s_add_u32 s96, s96, 8192\n"
        "s_cmp_eq_u32 s96, s97\n"
        "s_cselect_b32 s96, s98, s96\n"
        "s_mov_b32 m0, s99\n"
        "s_add_u32 s96, s96, 32768\n"
        "s_sub_u32 s66, s96, 704512\n"
        "s_cmp_ge_u32 s96, s97\n"
        "s_cselect_b32 s96, s66, s96\n"
        "s_add_u32 s99, s99, 8192\n"
        "buffer_load_dwordx4 v20, s[28:31], s68 offen offset:0 lds\n"
        "buffer_load_dwordx4 v20, s[28:31], s69 offen offset:1024 lds\n"
        "buffer_load_dwordx4 v20, s[28:31], s70 offen offset:2048 lds\n"
        "buffer_load_dwordx4 v20, s[28:31], s71 offen offset:3072 lds\n"
        "s_add_u32 s68, s96, 4096\n"
        "s_add_u32 s96, s96, 8192\n"
        "s_cmp_eq_u32 s96, s97\n"
        "s_cselect_b32 s96, s98, s96\n"
        "s_add_u32 s69, s96, 3072\n"
        "s_add_u32 s96, s96, 8192\n"
        "s_cmp_eq_u32 s96, s97\n"
        "s_cselect_b32 s96, s98, s96\n"
        "s_add_u32 s70, s96, 2048\n"
        "s_add_u32 s96, s96, 8192\n"
        "s_cmp_eq_u32 s96, s97\n"
        "s_cselect_b32 s96, s98, s96\n"
        "s_add_u32 s71, s96, 1024\n"
        "s_add_u32 s96, s96, 8192\n"
        "s_cmp_eq_u32 s96, s97\n"
        "s_cselect_b32 s96, s98, s96\n"
        "s_mov_b32 m0, s99\n"
        "s_add_u32 s96, s96, 32768\n"
        "s_sub_u32 s66, s96, 704512\n"
        "s_cmp_ge_u32 s96, s97\n"
        "s_cselect_b32 s96, s66, s96\n"
        "s_add_u32 s99, s99, 8192\n"
        "buffer_load_dwordx4 v20, s[28:31], s68 offen offset:0 lds\n"
        "buffer_load_dwordx4 v20, s[28:31], s69 offen offset:1024 lds\n"
        "buffer_load_dwordx4 v20, s[28:31], s70 offen offset:2048 lds\n"
        "buffer_load_dwordx4 v20, s[28:31], s71 offen offset:3072 lds\n"
        "s_add_u32 s68, s96, 4096\n"
        "s_add_u32 s96, s96, 8192\n"
        "s_cmp_eq_u32 s96, s97\n"
        "s_cselect_b32 s96, s98, s96\n"
        "s_add_u32 s69, s96, 3072\n"
        "s_add_u32 s96, s96, 8192\n"
        "s_cmp_eq_u32 s96, s97\n"
        "s_cselect_b32 s96, s98, s96\n"
        "s_add_u32 s70, s96, 2048\n"
        "s_add_u32 s96, s96, 8192\n"
        "s_cmp_eq_u32 s96, s97\n"
        "s_cselect_b32 s96, s98, s96\n"
        "s_add_u32 s71, s96, 1024\n"
        "s_add_u32 s96, s96, 8192\n"
        "s_cmp_eq_u32 s96, s97\n"
        "s_cselect_b32 s96, s98, s96\n"
        "s_mov_b32 m0, s99\n"
        "s_add_u32 s96, s96, 32768\n"
        "s_sub_u32 s66, s96, 704512\n"
        "s_cmp_ge_u32 s96, s97\n"
        "s_cselect_b32 s96, s66, s96\n"
        "s_add_u32 s99, s99, 8192\n"
        "buffer_load_dwordx4 v20, s[28:31], s68 offen offset:0 lds\n"
        "buffer_load_dwordx4 v20, s[28:31], s69 offen offset:1024 lds\n"
        "buffer_load_dwordx4 v20, s[28:31], s70 offen offset:2048 lds\n"
        "buffer_load_dwordx4 v20, s[28:31], s71 offen offset:3072 lds\n"
        "s_waitcnt vmcnt(20)\n"
        "s_barrier\n"
        "s_add_u32 s68, s96, 4096\n"
        "s_add_u32 s96, s96, 8192\n"
        "s_cmp_eq_u32 s96, s97\n"
        "s_cselect_b32 s96, s98, s96\n"
        "s_add_u32 s69, s96, 3072\n"
        "s_add_u32 s96, s96, 8192\n"
        "s_cmp_eq_u32 s96, s97\n"
        "s_cselect_b32 s96, s98, s96\n"
        "s_add_u32 s70, s96, 2048\n"
        "s_add_u32 s96, s96, 8192\n"
        "s_cmp_eq_u32 s96, s97\n"
        "s_cselect_b32 s96, s98, s96\n"
        "s_add_u32 s71, s96, 1024\n"
        "s_add_u32 s96, s96, 8192\n"
        "s_cmp_eq_u32 s96, s97\n"
        "s_cselect_b32 s96, s98, s96\n"
        "s_mov_b32 m0, s99\n"
        "s_add_u32 s96, s96, 32768\n"
        "s_sub_u32 s66, s96, 704512\n"
        "s_cmp_ge_u32 s96, s97\n"
        "s_cselect_b32 s96, s66, s96\n"
        "s_add_u32 s99, s99, 8192\n"
        "buffer_load_dwordx4 v20, s[28:31], s68 offen offset:0 lds\n"
        "buffer_load_dwordx4 v20, s[28:31], s69 offen offset:1024 lds\n"
        "buffer_load_dwordx4 v20, s[28:31], s70 offen offset:2048 lds\n"
        "buffer_load_dwordx4 v20, s[28:31], s71 offen offset:3072 lds\n"
        "s_lshl_b32 s67, s49, 2\n"
        "s_lshl_b32 s64, s67, 3\n"
        "s_add_u32 s64, s64, s78\n"
        "s_sub_u32 s64, s64, -48\n"
        "s_sub_u32 s66, s64, 688\n"
        "s_cselect_b32 s64, s64, s66\n"
        "s_lshl_b32 s96, s64, 10\n"
        "s_add_u32 s98, s79, 0\n"
        "s_add_u32 s96, s96, s98\n"
        "s_add_u32 s97, s98, 704512\n"
        "s_lshl_b32 s99, s67, 10\n"
        "s_add_u32 s68, s96, 4096\n"
        "s_add_u32 s96, s96, 8192\n"
        "s_cmp_eq_u32 s96, s97\n"
        "s_cselect_b32 s96, s98, s96\n"
        "s_add_u32 s69, s96, 3072\n"
        "s_add_u32 s96, s96, 8192\n"
        "s_cmp_eq_u32 s96, s97\n"
        "s_cselect_b32 s96, s98, s96\n"
        "s_add_u32 s70, s96, 2048\n"
        "s_add_u32 s96, s96, 8192\n"
        "s_cmp_eq_u32 s96, s97\n"
        "s_cselect_b32 s96, s98, s96\n"
        "s_add_u32 s71, s96, 1024\n"
        "s_add_u32 s96, s96, 8192\n"
        "s_cmp_eq_u32 s96, s97\n"
        "s_cselect_b32 s96, s98, s96\n"
        "s_mov_b32 m0, s99\n"
        "s_add_u32 s96, s96, 32768\n"
        "s_sub_u32 s66, s96, 704512\n"
        "s_cmp_ge_u32 s96, s97\n"
        "s_cselect_b32 s96, s66, s96\n"
        "s_add_u32 s99, s99, 8192\n"
        "buffer_load_dwordx4 v20, s[28:31], s68 offen offset:0 lds\n"
        "buffer_load_dwordx4 v20, s[28:31], s69 offen offset:1024 lds\n"
        "buffer_load_dwordx4 v20, s[28:31], s70 offen offset:2048 lds\n"
        "buffer_load_dwordx4 v20, s[28:31], s71 offen offset:3072 lds\n"
        "s_barrier\n"
        "s_add_u32 s68, s96, 4096\n"
        "s_add_u32 s96, s96, 8192\n"
        "s_cmp_eq_u32 s96, s97\n"
        "s_cselect_b32 s96, s98, s96\n"
        "s_add_u32 s69, s96, 3072\n"
        "s_add_u32 s96, s96, 8192\n"
        "s_cmp_eq_u32 s96, s97\n"
        "s_cselect_b32 s96, s98, s96\n"
        "s_add_u32 s70, s96, 2048\n"
        "s_add_u32 s96, s96, 8192\n"
        "s_cmp_eq_u32 s96, s97\n"
        "s_cselect_b32 s96, s98, s96\n"
        "s_add_u32 s71, s96, 1024\n"
        "s_add_u32 s96, s96, 8192\n"
        "s_cmp_eq_u32 s96, s97\n"
        "s_cselect_b32 s96, s98, s96\n"
        "s_mov_b32 m0, s99\n"
        "s_add_u32 s96, s96, 32768\n"
        "s_sub_u32 s66, s96, 704512\n"
        "s_cmp_ge_u32 s96, s97\n"
        "s_cselect_b32 s96, s66, s96\n"
        "s_add_u32 s99, s99, 8192\n"
        "buffer_load_dwordx4 v20, s[28:31], s68 offen offset:0 lds\n"
        "buffer_load_dwordx4 v20, s[28:31], s69 offen offset:1024 lds\n"
        "buffer_load_dwordx4 v20, s[28:31], s70 offen offset:2048 lds\n"
        "buffer_load_dwordx4 v20, s[28:31], s71 offen offset:3072 lds\n"
        "s_waitcnt vmcnt(8)\n"
        "s_add_u32 s68, s96, 4096\n"
        "s_add_u32 s96, s96, 8192\n"
        "s_cmp_eq_u32 s96, s97\n"
        "s_cselect_b32 s96, s98, s96\n"
        "s_add_u32 s69, s96, 3072\n"
        "s_add_u32 s96, s96, 8192\n"
        "s_cmp_eq_u32 s96, s97\n"
        "s_cselect_b32 s96, s98, s96\n"
        "s_add_u32 s70, s96, 2048\n"
        "s_add_u32 s96, s96, 8192\n"
        "s_cmp_eq_u32 s96, s97\n"
        "s_cselect_b32 s96, s98, s96\n"
        "s_add_u32 s71, s96, 1024\n"
        "s_add_u32 s96, s96, 8192\n"
        "s_cmp_eq_u32 s96, s97\n"
        "s_cselect_b32 s96, s98, s96\n"
        "s_mov_b32 m0, s99\n"
        "s_add_u32 s96, s96, 32768\n"
        "s_sub_u32 s66, s96, 704512\n"
        "s_cmp_ge_u32 s96, s97\n"
        "s_cselect_b32 s96, s66, s96\n"
        "s_add_u32 s99, s99, 8192\n"
        "buffer_load_dwordx4 v20, s[28:31], s68 offen offset:0 lds\n"
        "buffer_load_dwordx4 v20, s[28:31], s69 offen offset:1024 lds\n"
        "buffer_load_dwordx4 v20, s[28:31], s70 offen offset:2048 lds\n"
        "buffer_load_dwordx4 v20, s[28:31], s71 offen offset:3072 lds\n"
        "s_waitcnt vmcnt(8)\n"
        "s_add_u32 s68, s96, 4096\n"
        "s_add_u32 s96, s96, 8192\n"
        "s_cmp_eq_u32 s96, s97\n"
        "s_cselect_b32 s96, s98, s96\n"
        "s_add_u32 s69, s96, 3072\n"
        "s_add_u32 s96, s96, 8192\n"
        "s_cmp_eq_u32 s96, s97\n"
        "s_cselect_b32 s96, s98, s96\n"
        "s_add_u32 s70, s96, 2048\n"
        "s_add_u32 s96, s96, 8192\n"
        "s_cmp_eq_u32 s96, s97\n"
        "s_cselect_b32 s96, s98, s96\n"
        "s_add_u32 s71, s96, 1024\n"
        "s_add_u32 s96, s96, 8192\n"
        "s_cmp_eq_u32 s96, s97\n"
        "s_cselect_b32 s96, s98, s96\n"
        "s_mov_b32 m0, s99\n"
        "s_add_u32 s96, s96, 32768\n"
        "s_sub_u32 s66, s96, 704512\n"
        "s_cmp_ge_u32 s96, s97\n"
        "s_cselect_b32 s96, s66, s96\n"
        "s_add_u32 s99, s99, 8192\n"
        "buffer_load_dwordx4 v20, s[28:31], s68 offen offset:0 lds\n"
        "buffer_load_dwordx4 v20, s[28:31], s69 offen offset:1024 lds\n"
        "buffer_load_dwordx4 v20, s[28:31], s70 offen offset:2048 lds\n"
        "buffer_load_dwordx4 v20, s[28:31], s71 offen offset:3072 lds\n"
        "s_waitcnt vmcnt(8)\n"
        "s_add_u32 s68, s96, 4096\n"
        "s_add_u32 s96, s96, 8192\n"
        "s_cmp_eq_u32 s96, s97\n"
        "s_cselect_b32 s96, s98, s96\n"
        "s_add_u32 s69, s96, 3072\n"
        "s_add_u32 s96, s96, 8192\n"
        "s_cmp_eq_u32 s96, s97\n"
        "s_cselect_b32 s96, s98, s96\n"
        "s_add_u32 s70, s96, 2048\n"
        "s_add_u32 s96, s96, 8192\n"
        "s_cmp_eq_u32 s96, s97\n"
        "s_cselect_b32 s96, s98, s96\n"
        "s_add_u32 s71, s96, 1024\n"
        "s_add_u32 s96, s96, 8192\n"
        "s_cmp_eq_u32 s96, s97\n"
        "s_cselect_b32 s96, s98, s96\n"
        "s_mov_b32 m0, s99\n"
        "s_add_u32 s96, s96, 32768\n"
        "s_sub_u32 s66, s96, 704512\n"
        "s_cmp_ge_u32 s96, s97\n"
        "s_cselect_b32 s96, s66, s96\n"
        "s_add_u32 s99, s99, 8192\n"
        "buffer_load_dwordx4 v20, s[28:31], s68 offen offset:0 lds\n"
        "buffer_load_dwordx4 v20, s[28:31], s69 offen offset:1024 lds\n"
        "buffer_load_dwordx4 v20, s[28:31], s70 offen offset:2048 lds\n"
        "buffer_load_dwordx4 v20, s[28:31], s71 offen offset:3072 lds\n"
        "s_waitcnt vmcnt(8)\n"
        "s_add_u32 s68, s96, 4096\n"
        "s_add_u32 s96, s96, 8192\n"
        "s_cmp_eq_u32 s96, s97\n"
        "s_cselect_b32 s96, s98, s96\n"
        "s_add_u32 s69, s96, 3072\n"
        "s_add_u32 s96, s96, 8192\n"
        "s_cmp_eq_u32 s96, s97\n"
        "s_cselect_b32 s96, s98, s96\n"
        "s_add_u32 s70, s96, 2048\n"
        "s_add_u32 s96, s96, 8192\n"
        "s_cmp_eq_u32 s96, s97\n"
        "s_cselect_b32 s96, s98, s96\n"
        "s_add_u32 s71, s96, 1024\n"
        "s_add_u32 s96, s96, 8192\n"
        "s_cmp_eq_u32 s96, s97\n"
        "s_cselect_b32 s96, s98, s96\n"
        "s_mov_b32 m0, s99\n"
        "s_add_u32 s96, s96, 32768\n"
        "s_sub_u32 s66, s96, 704512\n"
        "s_cmp_ge_u32 s96, s97\n"
        "s_cselect_b32 s96, s66, s96\n"
        "s_add_u32 s99, s99, 8192\n"
        "buffer_load_dwordx4 v20, s[28:31], s68 offen offset:0 lds\n"
        "buffer_load_dwordx4 v20, s[28:31], s69 offen offset:1024 lds\n"
        "buffer_load_dwordx4 v20, s[28:31], s70 offen offset:2048 lds\n"
        "buffer_load_dwordx4 v20, s[28:31], s71 offen offset:3072 lds\n"
        "s_waitcnt vmcnt(8)\n"
        "s_add_u32 s68, s96, 4096\n"
        "s_add_u32 s96, s96, 8192\n"
        "s_cmp_eq_u32 s96, s97\n"
        "s_cselect_b32 s96, s98, s96\n"
        "s_add_u32 s69, s96, 3072\n"
        "s_add_u32 s96, s96, 8192\n"
        "s_cmp_eq_u32 s96, s97\n"
        "s_cselect_b32 s96, s98, s96\n"
        "s_add_u32 s70, s96, 2048\n"
        "s_add_u32 s96, s96, 8192\n"
        "s_cmp_eq_u32 s96, s97\n"
        "s_cselect_b32 s96, s98, s96\n"
        "s_add_u32 s71, s96, 1024\n"
        "s_add_u32 s96, s96, 8192\n"
        "s_cmp_eq_u32 s96, s97\n"
        "s_cselect_b32 s96, s98, s96\n"
        "s_mov_b32 m0, s99\n"
        "s_add_u32 s96, s96, 32768\n"
        "s_sub_u32 s66, s96, 704512\n"
        "s_cmp_ge_u32 s96, s97\n"
        "s_cselect_b32 s96, s66, s96\n"
        "s_add_u32 s99, s99, 8192\n"
        "buffer_load_dwordx4 v20, s[28:31], s68 offen offset:0 lds\n"
        "buffer_load_dwordx4 v20, s[28:31], s69 offen offset:1024 lds\n"
        "buffer_load_dwordx4 v20, s[28:31], s70 offen offset:2048 lds\n"
        "buffer_load_dwordx4 v20, s[28:31], s71 offen offset:3072 lds\n"
        "s_waitcnt vmcnt(8)\n"
        "s_add_u32 s68, s96, 4096\n"
        "s_add_u32 s96, s96, 8192\n"
        "s_cmp_eq_u32 s96, s97\n"
        "s_cselect_b32 s96, s98, s96\n"
        "s_add_u32 s69, s96, 3072\n"
        "s_add_u32 s96, s96, 8192\n"
        "s_cmp_eq_u32 s96, s97\n"
        "s_cselect_b32 s96, s98, s96\n"
        "s_add_u32 s70, s96, 2048\n"
        "s_add_u32 s96, s96, 8192\n"
        "s_cmp_eq_u32 s96, s97\n"
        "s_cselect_b32 s96, s98, s96\n"
        "s_add_u32 s71, s96, 1024\n"
        "s_add_u32 s96, s96, 8192\n"
        "s_cmp_eq_u32 s96, s97\n"
        "s_cselect_b32 s96, s98, s96\n"
        "s_mov_b32 m0, s99\n"
        "s_add_u32 s96, s96, 32768\n"
        "s_sub_u32 s66, s96, 704512\n"
        "s_cmp_ge_u32 s96, s97\n"
        "s_cselect_b32 s96, s66, s96\n"
        "s_add_u32 s99, s99, 8192\n"
        "buffer_load_dwordx4 v20, s[28:31], s68 offen offset:0 lds\n"
        "buffer_load_dwordx4 v20, s[28:31], s69 offen offset:1024 lds\n"
        "buffer_load_dwordx4 v20, s[28:31], s70 offen offset:2048 lds\n"
        "buffer_load_dwordx4 v20, s[28:31], s71 offen offset:3072 lds\n"
        "s_waitcnt vmcnt(8)\n"
        "s_add_u32 s68, s96, 4096\n"
        "s_add_u32 s96, s96, 8192\n"
        "s_cmp_eq_u32 s96, s97\n"
        "s_cselect_b32 s96, s98, s96\n"
        "s_add_u32 s69, s96, 3072\n"
        "s_add_u32 s96, s96, 8192\n"
        "s_cmp_eq_u32 s96, s97\n"
        "s_cselect_b32 s96, s98, s96\n"
        "s_add_u32 s70, s96, 2048\n"
        "s_add_u32 s96, s96, 8192\n"
        "s_cmp_eq_u32 s96, s97\n"
        "s_cselect_b32 s96, s98, s96\n"
        "s_add_u32 s71, s96, 1024\n"
        "s_add_u32 s96, s96, 8192\n"
        "s_cmp_eq_u32 s96, s97\n"
        "s_cselect_b32 s96, s98, s96\n"
        "s_mov_b32 m0, s99\n"
        "s_add_u32 s96, s96, 32768\n"
        "s_sub_u32 s66, s96, 704512\n"
        "s_cmp_ge_u32 s96, s97\n"
        "s_cselect_b32 s96, s66, s96\n"
        "s_add_u32 s99, s99, 8192\n"
        "buffer_load_dwordx4 v20, s[28:31], s68 offen offset:0 lds\n"
        "buffer_load_dwordx4 v20, s[28:31], s69 offen offset:1024 lds\n"
        "buffer_load_dwordx4 v20, s[28:31], s70 offen offset:2048 lds\n"
        "buffer_load_dwordx4 v20, s[28:31], s71 offen offset:3072 lds\n"
        "s_waitcnt vmcnt(8)\n"
        "s_add_u32 s68, s96, 4096\n"
        "s_add_u32 s96, s96, 8192\n"
        "s_cmp_eq_u32 s96, s97\n"
        "s_cselect_b32 s96, s98, s96\n"
        "s_add_u32 s69, s96, 3072\n"
        "s_add_u32 s96, s96, 8192\n"
        "s_cmp_eq_u32 s96, s97\n"
        "s_cselect_b32 s96, s98, s96\n"
        "s_add_u32 s70, s96, 2048\n"
        "s_add_u32 s96, s96, 8192\n"
        "s_cmp_eq_u32 s96, s97\n"
        "s_cselect_b32 s96, s98, s96\n"
        "s_add_u32 s71, s96, 1024\n"
        "s_add_u32 s96, s96, 8192\n"
        "s_cmp_eq_u32 s96, s97\n"
        "s_cselect_b32 s96, s98, s96\n"
        "s_mov_b32 m0, s99\n"
        "s_add_u32 s96, s96, 32768\n"
        "s_sub_u32 s66, s96, 704512\n"
        "s_cmp_ge_u32 s96, s97\n"
        "s_cselect_b32 s96, s66, s96\n"
        "s_add_u32 s99, s99, 8192\n"
        "buffer_load_dwordx4 v20, s[28:31], s68 offen offset:0 lds\n"
        "buffer_load_dwordx4 v20, s[28:31], s69 offen offset:1024 lds\n"
        "buffer_load_dwordx4 v20, s[28:31], s70 offen offset:2048 lds\n"
        "buffer_load_dwordx4 v20, s[28:31], s71 offen offset:3072 lds\n"
        "s_waitcnt vmcnt(8)\n"
        "s_mul_i32 s67, s49, 6\n"
        "s_add_u32 s67, s67, 148\n"
        "s_lshl_b32 s64, s67, 3\n"
        "s_add_u32 s64, s64, s78\n"
        "s_sub_u32 s64, s64, 592\n"
        "s_sub_u32 s66, s64, 688\n"
        "s_cselect_b32 s64, s64, s66\n"
        "s_lshl_b32 s96, s64, 10\n"
        "s_add_u32 s98, s79, 704512\n"
        "s_add_u32 s96, s96, s98\n"
        "s_add_u32 s97, s98, 704512\n"
        "s_lshl_b32 s99, s67, 10\n"
        "s_add_u32 s68, s96, 4096\n"
        "s_add_u32 s96, s96, 8192\n"
        "s_cmp_eq_u32 s96, s97\n"
        "s_cselect_b32 s96, s98, s96\n"
        "s_add_u32 s69, s96, 3072\n"
        "s_add_u32 s96, s96, 8192\n"
        "s_cmp_eq_u32 s96, s97\n"
        "s_cselect_b32 s96, s98, s96\n"
        "s_add_u32 s70, s96, 2048\n"
        "s_add_u32 s96, s96, 8192\n"
        "s_cmp_eq_u32 s96, s97\n"
        "s_cselect_b32 s96, s98, s96\n"
        "s_add_u32 s71, s96, 1024\n"
        "s_add_u32 s96, s96, 8192\n"
        "s_cmp_eq_u32 s96, s97\n"
        "s_cselect_b32 s96, s98, s96\n"
        "s_mov_b32 m0, s99\n"
        "s_nop 0\n"
        "s_add_u32 s99, s99, 4096\n"
        "buffer_load_dwordx4 v20, s[28:31], s68 offen offset:0 lds\n"
        "buffer_load_dwordx4 v20, s[28:31], s69 offen offset:1024 lds\n"
        "buffer_load_dwordx4 v20, s[28:31], s70 offen offset:2048 lds\n"
        "buffer_load_dwordx4 v20, s[28:31], s71 offen offset:3072 lds\n"
        "s_waitcnt vmcnt(8)\n"
        "s_add_u32 s68, s96, 4096\n"
        "s_add_u32 s96, s96, 8192\n"
        "s_cmp_eq_u32 s96, s97\n"
        "s_cselect_b32 s96, s98, s96\n"
        "s_add_u32 s69, s96, 3072\n"
        "s_add_u32 s96, s96, 8192\n"
        "s_cmp_eq_u32 s96, s97\n"
        "s_cselect_b32 s96, s98, s96\n"
        "s_mov_b32 m0, s99\n"
        "s_nop 0\n"
        "s_add_u32 s99, s99, 2048\n"
        "buffer_load_dwordx4 v20, s[28:31], s68 offen offset:0 lds\n"
        "buffer_load_dwordx4 v20, s[28:31], s69 offen offset:1024 lds\n"
        "s_lshl_b32 s67, s49, 1\n"
        "s_add_u32 s67, s67, 80\n"
        "s_lshl_b32 s64, s67, 3\n"
        "s_add_u32 s64, s64, s78\n"
        "s_sub_u32 s64, s64, 592\n"
        "s_sub_u32 s66, s64, 688\n"
        "s_cselect_b32 s64, s64, s66\n"
        "s_lshl_b32 s96, s64, 10\n"
        "s_add_u32 s98, s79, 704512\n"
        "s_add_u32 s96, s96, s98\n"
        "s_add_u32 s97, s98, 704512\n"
        "s_lshl_b32 s99, s67, 10\n"
        "s_add_u32 s68, s96, 4096\n"
        "s_add_u32 s96, s96, 8192\n"
        "s_cmp_eq_u32 s96, s97\n"
        "s_cselect_b32 s96, s98, s96\n"
        "s_add_u32 s69, s96, 3072\n"
        "s_add_u32 s96, s96, 8192\n"
        "s_cmp_eq_u32 s96, s97\n"
        "s_cselect_b32 s96, s98, s96\n"
        "s_mov_b32 m0, s99\n"
        "s_nop 0\n"
        "s_add_u32 s99, s99, 2048\n"
        "buffer_load_dwordx4 v20, s[28:31], s68 offen offset:0 lds\n"
        "buffer_load_dwordx4 v20, s[28:31], s69 offen offset:1024 lds\n"
        "s_cmp_lg_u32 %1, 0\n"
        "s_cbranch_scc1 Llend_%=\n"
        "s_cmp_lg_u32 s48, 5\n"
        "s_cbranch_scc1 Llend_%=\n"
        "buffer_load_dwordx2 v[20:21], off, s[40:43], 0 sc1\n"
        "v_lshlrev_b32 v2, 5, v1\n"
        "v_add_u32 v2, 4096, v2\n"
        "s_waitcnt vmcnt(0)\n"
        "v_readfirstlane_b32 s64, v20\n"
        "v_readfirstlane_b32 s65, v21\n"
        "s_mov_b32 s90, 200000\n"
        "s_nop 3\n"
        "s_mul_i32 s60, s64, 0x4C957F2D\n"
        "s_mul_hi_u32 s66, s64, 0x4C957F2D\n"
        "s_mul_i32 s67, s64, 0x5851F42D\n"
        "s_add_u32 s66, s66, s67\n"
        "s_mul_i32 s67, s65, 0x4C957F2D\n"
        "s_add_u32 s66, s66, s67\n"
        "s_add_u32 s60, s60, 0xF767814F\n"
        "s_addc_u32 s61, s66, 0x14057B7E\n"
        "Lkpoll_%=:\n"
        "buffer_load_dwordx4 v[24:27], v2, s[40:43], 0 offen sc1\n"
        "buffer_load_dwordx4 v[28:31], v2, s[40:43], 0 offen offset:16 sc1\n"
        "s_waitcnt vmcnt(0)\n"
        "v_xor_b32 v24, s60, v24\n"
        "v_xor_b32 v25, s61, v25\n"
        "v_xor_b32 v26, s60, v26\n"
        "v_xor_b32 v27, s61, v27\n"
        "v_xor_b32 v28, s60, v28\n"
        "v_xor_b32 v29, s61, v29\n"
        "v_xor_b32 v30, s60, v30\n"
        "v_xor_b32 v31, s61, v31\n"
        "v_or3_b32 v24, v24, v25, v26\n"
        "v_or3_b32 v27, v27, v28, v29\n"
        "v_or3_b32 v24, v24, v30, v31\n"
        "v_or_b32 v24, v24, v27\n"
        "v_cmp_eq_u32 vcc, 0, v24\n"
        "s_nop 1\n"
        "s_cmp_eq_u64 vcc, -1\n"
        "s_cbranch_scc1 Lkdone_%=\n"
        "s_sub_u32 s90, s90, 1\n"
        "s_cbranch_scc1 Llend_%=\n"
        "s_sleep 8\n"
        "s_branch Lkpoll_%=\n"
        "Lkdone_%=:\n"
        "v_mov_b32 v20, s60\n"
        "v_mov_b32 v21, s61\n"
        "s_mov_b64 exec, 1\n"
        "buffer_store_dwordx2 v[20:21], off, s[40:43], 0 sc1\n"
        "s_mov_b64 exec, -1\n"
        "Llend_%=:\n"
        "s_waitcnt vmcnt(0)\n"
        "s_barrier\n"
        "s_endpgm\n"
        :
        : "s"(__builtin_amdgcn_kernarg_segment_ptr()), "s"(blockIdx.x), "v"(threadIdx.x)
        : "s3", "s4", "s5", "s6", "s7", "s8", "s9", "s10", "s11", "s12", "s13", "s14", "s15", "s16", "s17", "s18", "s19", "s20", "s21", "s22", "s23", "s24", "s25", "s26", "s27", "s28", "s29", "s30", "s31", "s33", "s34", "s35", "s36", "s37", "s38", "s39", "s40", "s41", "s42", "s43", "s44", "s45", "s46", "s47", "s48", "s49", "s50", "s51", "s52", "s53", "s54", "s55", "s56", "s57", "s58", "s59", "s60", "s61", "s62", "s63", "s64", "s65", "s66", "s67", "s68", "s69", "s70", "s71", "s72", "s73", "s74", "s75", "s76", "s77", "s78", "s79", "s80", "s81", "s82", "s83", "s84", "s85", "s86", "s87", "s88", "s89", "s90", "s91", "s92", "s93", "s94", "s95", "s96", "s97", "s98", "s99", "v1", "v2", "v3", "v4", "v5", "v6", "v7", "v8", "v9", "v10", "v11", "v12", "v13", "v14", "v15", "v16", "v17", "v18", "v19", "v20", "v21", "v22", "v23", "v24", "v25", "v26", "v27", "v28", "v29", "v30", "v31", "v32", "v33", "v34", "v35", "v36", "v37", "v38", "v39", "v40", "v41", "v42", "v43", "v44", "v45", "v46", "v47", "v48", "v49", "v50", "v51", "v52", "v53", "v54", "v55", "v56", "v57", "v58", "v59", "v60", "v61", "v62", "v63", "v64", "v65", "v66", "v67", "v68", "v69", "v70", "v71", "v72", "v73", "v74", "v75", "v76", "v77", "v78", "v79", "v80", "v81", "v82", "v83", "v84", "v85", "v86", "v87", "v88", "v89", "v90", "v91", "v92", "v93", "v94", "v95", "v96", "v97", "v98", "v99", "v100", "v101", "v102", "v103", "v104", "v105", "v106", "v107", "v108", "v109", "v110", "v111", "v112", "v113", "v114", "v115", "v116", "v117", "v118", "v119", "v120", "v121", "v122", "v123", "v124", "v125", "v126", "v127", "v128", "v129", "v130", "v131", "v132", "v133", "v134", "v135", "v136", "v137", "v138", "v139", "v140", "v141", "v142", "v143", "v144", "v145", "v146", "v147", "v148", "v149", "v150", "v151", "v152", "v153", "v154", "v155", "v156", "v157", "v158", "v159", "v160", "v161", "v162", "v163", "v164", "v165", "v166", "v167", "v168", "v169", "v170", "v171", "v172", "v173", "v174", "v175", "v176", "v177", "v178", "v179", "v180", "v181", "v182", "v183", "v184", "v185", "v186", "v187", "v188", "v189", "v190", "v191", "v192", "v193", "v194", "v195", "v196", "v197", "v198", "v199", "v200", "v201", "v202", "v203", "v204", "v205", "v206", "v207", "v208", "v209", "v210", "v211", "v212", "v213", "v214", "v215", "v216", "v217", "v218", "v219", "v220", "v221", "v222", "v223", "v224", "v225", "v226", "v227", "v228", "v229", "v230", "v231", "v232", "v233", "v234", "v235", "v236", "v237", "v238", "v239", "v240", "v241", "v242", "v243", "v244", "v245", "v246", "v247", "v248", "v249", "v250", "v251", "v252", "v253", "v254", "v255", "vcc", "scc", "memory");
}

extern "C" void kernel_launch(void* const* d_in, const int* in_sizes, int n_in, void* d_out, int out_size, void* d_ws, size_t ws_size,
                              hipStream_t stream) {
    (void)in_sizes; (void)n_in; (void)out_size; (void)ws_size;
    (void)hipFuncSetAttribute((const void*)mlp_fused, hipFuncAttributeMaxDynamicSharedMemorySize, 163840);
    mlp_fused<<<256, 384, 163840, stream>>>(d_in[0], d_in[1], d_in[2], d_in[3], d_in[4], d_out, d_ws);
}
```
